```python
import torch

def _softmax(x, axis=-1):
    return torch.softmax(x, dim=axis)
def _gelu(x):
    return torch.nn.functional.gelu(x, approximate="tanh")
def _silu(x):
    return torch.nn.functional.silu(x)
def _sigmoid(x):
    return torch.sigmoid(x)
def _layernorm(x, axis=-1, eps=1e-5):
    m = x.mean(dim=axis, keepdim=True)
    v = x.var(dim=axis, keepdim=True, unbiased=False)
    return (x - m) / torch.sqrt(v + eps)
def _scatter_add(tgt, idx, src, axis=0):
    return tgt.clone().index_add_(axis, idx, src)
def _segment_sum(src, seg, n):
    out = torch.zeros((n,) + src.shape[1:], dtype=src.dtype)
    return out.index_add_(0, seg, src)
def _pad(x, pw):
    flat = tuple(v for pair in reversed(pw) for v in pair)
    return torch.nn.functional.pad(x, flat)

kernel_name = 'gated_mix__ssm_conv_h3__geglu_ffn__prompt'

def setup_inputs():

    _w = (lambda a: (lambda t: t.to(torch.bfloat16) if t.is_floating_point() else t)(torch.as_tensor(a)))
    def _A_setup():
        B, S, D = 1, 2048, 1024
        ks = 4
        return {
            "q": _w((lambda *s: torch.randn(*s, dtype=torch.bfloat16) * 0.05)(B, S, D)),
            "k": _w((lambda *s: torch.randn(*s, dtype=torch.bfloat16) * 0.4)(B, S, D)),
            "v": _w((lambda *s: torch.randn(*s, dtype=torch.bfloat16) * 0.4)(B, S, D)),
            "shift_k": _w((lambda *s: torch.randn(*s, dtype=torch.bfloat16) * 0.4)(D, ks) * 0.5),
            "ssm_k": _w((lambda *s: torch.randn(*s, dtype=torch.bfloat16) * 0.4)(D, S) * (1.0 / S ** 0.5)),
            "ssm_D": _w((lambda *s: torch.randn(*s, dtype=torch.bfloat16) * 0.4)(D)),
        }
    _A_in = _A_setup()

    def _B_setup():
        B, S, D, F = 1, 2048, 1024, 3584
        return {
            "x": _w((lambda *s: torch.randn(*s, dtype=torch.bfloat16) * 0.05)(B, S, D)),
            "w_gate": _w((lambda *s: torch.randn(*s, dtype=torch.bfloat16) * 0.05)(D, F)),
            "w_up": _w((lambda *s: torch.randn(*s, dtype=torch.bfloat16) * 0.05)(D, F)),
            "w_down": _w((lambda *s: torch.randn(*s, dtype=torch.bfloat16) * 0.05)(F, D)),
        }
    _B_in = _B_setup()

    return {
        "A_q": _A_in["q"],
        "A_k": _A_in["k"],
        "A_v": _A_in["v"],
        "A_shift_k": _A_in["shift_k"],
        "A_ssm_k": _A_in["ssm_k"],
        "A_ssm_D": _A_in["ssm_D"],
        "B_x": _B_in["x"],
        "B_w_gate": _B_in["w_gate"],
        "B_w_up": _B_in["w_up"],
        "B_w_down": _B_in["w_down"],
        "gate_w": _w((lambda *s: torch.randn(*s, dtype=torch.bfloat16) * 0.05)(1024) * 16.0),
    }

def reference(A_q, A_k, A_v, A_shift_k, A_ssm_k, A_ssm_D, B_x, B_w_gate, B_w_up, B_w_down, gate_w):
    def _A_ref(q, k, v, shift_k, ssm_k, ssm_D):
        B, S, D = q.shape
        ks = shift_k.shape[-1]
        k_pad = _pad(k, ((0, 0), (ks - 1, 0), (0, 0)))
        k_shift = torch.zeros_like(k)
        for t in range(ks):
            k_shift = k_shift + k_pad[:, t:t + S, :] * shift_k[:, ks - 1 - t][None, None, :]
        kv = k_shift * v
        L = 2 * S
        kv_pad = _pad(kv, ((0, 0), (0, S), (0, 0)))
        kf_pad = _pad(ssm_k, ((0, 0), (0, S)))
        kv_f = (lambda x, axes=(-2,-1): torch.fft.rfft2(x.float(), dim=axes))(kv_pad[:, None, :, :], axes=(1, 2))
        kf_f = (lambda x, axes=(-2,-1): torch.fft.rfft2(x.float(), dim=axes))(kf_pad[:, None, :], axes=(1, 2))
        kf_f = (lambda x, *d: x.permute(*d))(kf_f, 1, 2, 0)
        y_f = kv_f * kf_f[None, :, :, :]
        y = (lambda x, s, axes=(-2,-1): torch.fft.irfft2(x, s=s, dim=axes).to(torch.bfloat16))(y_f, (1, L), axes=(1, 2))[:, 0, :S, :]
        y = y + kv * ssm_D[None, None, :]
        return q * y

    def _B_ref(x, w_gate, w_up, w_down):
        return (_gelu(x @ w_gate) * (x @ w_up)) @ w_down

    x = A_q
    g = _sigmoid((x * gate_w).sum(-1, keepdims=True))
    a = _A_ref(x, A_k, A_v, A_shift_k, A_ssm_k, A_ssm_D)
    b = _B_ref(x, B_w_gate, B_w_up, B_w_down)
    return g * a + (1 - g) * b
```

```cpp
#include <hip/hip_runtime.h>
#include <hip/hip_bf16.h>
typedef unsigned short u16;
__device__ __forceinline__ float bf2f(u16 b){ return __uint_as_float(((unsigned)b)<<16); }
__device__ __forceinline__ u16 f2bf(float f){ unsigned u=__float_as_uint(f); u += 0x7FFFu + ((u>>16)&1u); return (u16)(u>>16); }
__device__ __forceinline__ float rb(float f){ return bf2f(f2bf(f)); }
constexpr int S=2048, D=1024, F=3584;

__global__ void k_kv(const u16* k,const u16* v,const u16* sh,u16* kv){
  int i=blockIdx.x*blockDim.x+threadIdx.x; if(i>=S*D) return; int s=i/D,d=i%D;
  float acc=0.f;
  for(int t=0;t<4;++t){ int ss=s+t-3; float kk= ss>=0? bf2f(k[ss*D+d]):0.f; acc=rb(acc+rb(kk*bf2f(sh[d*4+3-t]))); }
  kv[i]=f2bf(acc*bf2f(v[i]));
}
__global__ void k_g(const u16* x,const u16* gw,float* g){
  int s=blockIdx.x; float a=0.f; for(int d=threadIdx.x;d<D;d+=64) a+=rb(bf2f(x[s*D+d])*bf2f(gw[d]));
  for(int o=32;o>=1;o>>=1) a+=__shfl_xor(a,o);
  if(threadIdx.x==0){ a=rb(a); g[s]=rb(1.f/(1.f+__expf(-a))); }
}
__global__ void k_conv(const u16* kv,const u16* hk,const u16* Dv,const u16* q,u16* a){
  int d=blockIdx.x*64+(threadIdx.x&63); int s=blockIdx.y*4+(threadIdx.x>>6);
  float acc=0.f; for(int t=0;t<=s;++t) acc+=bf2f(kv[(s-t)*D+d])*bf2f(hk[d*S+t]);
  float y=rb(acc); y=rb(y+rb(bf2f(kv[s*D+d])*bf2f(Dv[d])));
  a[s*D+d]=f2bf(bf2f(q[s*D+d])*y);
}
__global__ void k_g1(const u16* x,const u16* wg,const u16* wu,u16* h){
  int f=blockIdx.x*64+(threadIdx.x&63); int s=blockIdx.y*4+(threadIdx.x>>6);
  float ag=0.f,au=0.f; for(int k=0;k<D;++k){ float xv=bf2f(x[s*D+k]); ag+=xv*bf2f(wg[k*F+f]); au+=xv*bf2f(wu[k*F+f]); }
  ag=rb(ag); au=rb(au);
  float u=0.7978845608f*(ag+0.044715f*ag*ag*ag); float ge=rb(0.5f*ag*(1.f+tanhf(u)));
  h[s*F+f]=f2bf(ge*au);
}
__global__ void k_g2(const u16* h,const u16* wd,const u16* a,const float* g,u16* out){
  int d=blockIdx.x*64+(threadIdx.x&63); int s=blockIdx.y*4+(threadIdx.x>>6);
  float acc=0.f; for(int k=0;k<F;++k) acc+=bf2f(h[s*F+k])*bf2f(wd[k*D+d]);
  float b=rb(acc); float gg=g[s];
  out[s*D+d]=f2bf(rb(gg*bf2f(a[s*D+d]))+rb(rb(1.f-gg)*b));
}
extern "C" void kernel_launch(void* const* d_in, const int* in_sizes, int n_in, void* d_out, int out_size, void* d_ws, size_t ws_size, hipStream_t stream){
  const u16* q=(const u16*)d_in[0]; const u16* k=(const u16*)d_in[1]; const u16* v=(const u16*)d_in[2];
  const u16* sh=(const u16*)d_in[3]; const u16* hk=(const u16*)d_in[4]; const u16* Dv=(const u16*)d_in[5];
  const u16* wg=(const u16*)d_in[7]; const u16* wu=(const u16*)d_in[8]; const u16* wd=(const u16*)d_in[9]; const u16* gw=(const u16*)d_in[10];
  char* ws=(char*)d_ws;
  u16* kv=(u16*)ws; u16* a=(u16*)(ws+(size_t)S*D*2); u16* h=(u16*)(ws+(size_t)S*D*4); float* g=(float*)(ws+(size_t)S*D*4+(size_t)S*F*2);
  k_kv<<<S*D/256,256,0,stream>>>(k,v,sh,kv);
  k_g<<<S,64,0,stream>>>(q,gw,g);
  k_conv<<<dim3(D/64,S/4),256,0,stream>>>(kv,hk,Dv,q,a);
  k_g1<<<dim3(F/64,S/4),256,0,stream>>>(q,wg,wu,h);
  k_g2<<<dim3(D/64,S/4),256,0,stream>>>(h,wd,a,g,(u16*)d_out);
}
```

```cpp
#include <hip/hip_runtime.h>
#include <hip/hip_bf16.h>

typedef unsigned short u16;
typedef short s16x4 __attribute__((ext_vector_type(4)));
typedef short s16x8 __attribute__((ext_vector_type(8)));
typedef __bf16 bfx2 __attribute__((ext_vector_type(2)));
typedef __bf16 bfx8 __attribute__((ext_vector_type(8)));
typedef float f32x4 __attribute__((ext_vector_type(4)));
typedef float f32x2 __attribute__((ext_vector_type(2)));
typedef unsigned u32x4 __attribute__((ext_vector_type(4)));
typedef unsigned u32x2 __attribute__((ext_vector_type(2)));
#define LAS __attribute__((address_space(3)))
#define DEV __device__ __forceinline__

constexpr int S_ = 2048, D_ = 1024, F_ = 3584;
constexpr size_t WS_HF = 0;
constexpr size_t WS_WDF = 16u << 20;
constexpr size_t WS_KVT = 24u << 20;
constexpr size_t WS_ZT = 28u << 20;
constexpr size_t WS_G = 32u << 20;

DEV float bflo(unsigned w) { return __uint_as_float(w << 16); }
DEV float bfhi(unsigned w) { return __uint_as_float(w & 0xffff0000u); }
DEV unsigned pack2(float lo, float hi) { bfx2 p; p[0] = (__bf16)lo; p[1] = (__bf16)hi; return __builtin_bit_cast(unsigned, p); }
DEV float rbf(float f) { return (float)(__bf16)f; }
DEV f32x4 mfma16(s16x8 a, s16x8 b, f32x4 c) {
  return __builtin_amdgcn_mfma_f32_16x16x32_bf16(__builtin_bit_cast(bfx8, a), __builtin_bit_cast(bfx8, b), c, 0, 0, 0);
}
DEV s16x8 tr_read8(LAS char* p0, LAS char* p1) {
  s16x4 lo = __builtin_amdgcn_ds_read_tr16_b64_v4i16((LAS s16x4*)p0);
  s16x4 hi = __builtin_amdgcn_ds_read_tr16_b64_v4i16((LAS s16x4*)p1);
  return __builtin_shufflevector(lo, hi, 0, 1, 2, 3, 4, 5, 6, 7);
}
DEV void glds16(const char* g, LAS char* l) {
  __builtin_amdgcn_global_load_lds((const __attribute__((address_space(1))) unsigned*)g, (LAS unsigned*)l, 16, 0, 0);
}
typedef __amdgpu_buffer_rsrc_t rsrc_t;
DEV rsrc_t mk_rsrc(const void* p, unsigned bytes) { return __builtin_amdgcn_make_buffer_rsrc((void*)p, 0, bytes, 0x00020000); }
DEV void blds16(rsrc_t r, LAS char* l, unsigned voff, unsigned soff) {
  __builtin_amdgcn_raw_ptr_buffer_load_lds(r, (LAS void*)l, 16, voff, soff, 0, 0);
}
#define STAMP_AT(i)
#define WAIT_V(n) asm volatile("s_waitcnt vmcnt(" #n ")" ::: "memory")
#define WAIT_L(n) asm volatile("s_waitcnt lgkmcnt(" #n ")" ::: "memory")
#define BAR() __builtin_amdgcn_s_barrier()
#define SCHED() __builtin_amdgcn_sched_barrier(0)

DEV void gemm1_tile(LAS char* lds, const u16* __restrict__ x, const u16* __restrict__ wg, const u16* __restrict__ wu,
                    char* __restrict__ hF, int pm, int pn) {
  const int tid = threadIdx.x, lane = tid & 63;
  const int wid = __builtin_amdgcn_readfirstlane(tid >> 6);
  const int wr = wid >> 1, wc = wid & 1;
  char* stamp_base = hF + (64u << 20);
  STAMP_AT(0);
  const int fr = lane & 15, fq = lane >> 4;
  unsigned va[2], vb[2];
#pragma unroll
  for (int e = 0; e < 2; ++e) {
    int rl = lane >> 3, p = lane & 7;
    int sw = (e << 2) | (lane >> 4);
    va[e] = (unsigned)(rl * 2048 + ((p ^ sw) * 16));
    int kl = lane >> 4, pb = lane & 15;
    int swb = (kl | (e << 2)) << 1;
    vb[e] = (unsigned)(kl * (F_ * 2) + ((pb ^ swb) * 16));
  }
  const rsrc_t rx = mk_rsrc(x, S_ * D_ * 2);
  const rsrc_t rw = mk_rsrc((wid < 4) ? wg : wu, D_ * F_ * 2);
  const unsigned xa_base = (unsigned)(pm * 256 + wid * 32) * 2048u;
  const unsigned wb_base = (unsigned)((wid & 3) * 16) * (F_ * 2) + pn * 256;
  LAS char* ldsA = lds + wid * 4096;
  LAS char* ldsB = lds + 65536 + wid * 4096;
  auto dmaA = [&](int kt, int st, int i) { blds16(rx, ldsA + st * 32768 + i * 1024, va[i & 1], xa_base + i * (8 * 2048) + kt * 128); };
  auto dmaB = [&](int kt, int st, int i) { blds16(rw, ldsB + st * 32768 + i * 1024, vb[i >> 1], wb_base + (kt * 64 + i * 4) * (F_ * 2)); };
  LAS char* bx[2];
#pragma unroll
  for (int ks = 0; ks < 2; ++ks) bx[ks] = lds + (wr * 64 + fr) * 128 + (((ks * 4 + fq) ^ (fr >> 1)) * 16);
  LAS char* bw[4];
  {
    int q = (lane & 15) >> 2, p = lane & 3;
    int sw = (q | ((fq & 1) << 2)) << 1;
#pragma unroll
    for (int j = 0; j < 4; ++j) bw[j] = lds + 65536 + (8 * fq + q) * 256 + (((wc * 8 + j * 2 + (p >> 1)) ^ sw) * 16) + (p & 1) * 8;
  }
  auto ldx = [&](int st, int ks, int i) -> s16x8 { return *(LAS s16x8*)(bx[ks] + st * 32768 + i * 2048); };
  auto ldw = [&](int st, int ks, int nf) -> s16x8 {
    LAS char* p = bw[nf & 3] + st * 32768 + (nf >> 2) * 16384 + ks * 8192;
    return tr_read8(p, p + 1024);
  };
  f32x4 acc[4][8];
#pragma unroll
  for (int i = 0; i < 4; ++i)
#pragma unroll
    for (int n = 0; n < 8; ++n) acc[i][n] = (f32x4){0.f, 0.f, 0.f, 0.f};
  s16x8 xa[4], xb[4], w[8];
#pragma unroll
  for (int i = 0; i < 4; ++i) { dmaA(0, 0, i); dmaB(0, 0, i); }
#pragma unroll
  for (int i = 0; i < 4; ++i) { dmaA(1, 1, i); dmaB(1, 1, i); }
  WAIT_V(8); BAR(); SCHED();
  STAMP_AT(1);
#pragma unroll
  for (int i = 0; i < 4; ++i) xa[i] = ldx(0, 0, i);
#pragma unroll
  for (int n = 0; n < 8; ++n) w[n] = ldw(0, 0, n);
  SCHED();

  auto tile_body = [&](const int st, const int kt, const bool pre1, const bool pre2) {
#pragma unroll
    for (int n = 0; n < 8; ++n) {
#pragma unroll
      for (int i = 0; i < 4; ++i) acc[i][n] = mfma16(w[n], xa[i], acc[i][n]);
      w[n] = ldw(st, 1, n);
      if (n < 4) xb[n] = ldx(st, 1, n);
      SCHED();
    }
#pragma unroll
    for (int n = 0; n < 2; ++n) {
#pragma unroll
      for (int i = 0; i < 4; ++i) acc[i][n] = mfma16(w[n], xb[i], acc[i][n]);
    }
    SCHED();
    if (pre1) {
      WAIT_L(0); WAIT_V(0); BAR(); SCHED();
      w[0] = ldw(st ^ 1, 0, 0); w[1] = ldw(st ^ 1, 0, 1);
      if (pre2) { dmaA(kt + 2, st, 0); dmaB(kt + 2, st, 0); }
      SCHED();
    }
#pragma unroll
    for (int n = 2; n < 8; ++n) {
#pragma unroll
      for (int i = 0; i < 4; ++i) acc[i][n] = mfma16(w[n], xb[i], acc[i][n]);
      if (pre1) {
        w[n] = ldw(st ^ 1, 0, n);
        if (n - 2 < 4) xa[n - 2] = ldx(st ^ 1, 0, n - 2);
        if (pre2 && n < 5) { dmaA(kt + 2, st, n - 1); dmaB(kt + 2, st, n - 1); }
      }
      SCHED();
    }
  };
  for (int kt = 0; kt < 14; kt += 2) {
    tile_body(0, kt, true, true);
    tile_body(1, kt + 1, true, true);
  }
  STAMP_AT(2);
  tile_body(0, 14, true, false);
  tile_body(1, 15, false, false);
  STAMP_AT(3);

  const float C1 = -2.302208198f, C2 = -2.302208198f * 0.044715f;
#pragma unroll
  for (int i = 0; i < 4; ++i) {
    const int mtile = pm * 16 + wr * 4 + i;
#pragma unroll
    for (int k2 = 0; k2 < 2; ++k2) {
      unsigned pk[4];
#pragma unroll
      for (int jj = 0; jj < 2; ++jj) {
        const f32x4 gt = acc[i][k2 * 2 + jj], up = acc[i][4 + k2 * 2 + jj];
        float hv[4];
#pragma unroll
        for (int r = 0; r < 4; ++r) {
          const float a = gt[r];
          const float e = __builtin_amdgcn_exp2f(a * (C1 + C2 * a * a));
          hv[r] = a * up[r] * __builtin_amdgcn_rcpf(1.0f + e);
        }
        pk[jj * 2] = pack2(hv[0], hv[1]); pk[jj * 2 + 1] = pack2(hv[2], hv[3]);
      }
      const int ktile = pn * 4 + wc * 2 + k2;
      *(u32x4*)(hF + ((size_t)(ktile * 128 + mtile) * 64 + lane) * 16) = (u32x4){pk[0], pk[1], pk[2], pk[3]};
    }
  }
  STAMP_AT(4);
  WAIT_V(0);
  STAMP_AT(7);
}

constexpr int CW_H = 0, CW_CS = 2112, CW_U = 4 * 2112  , CW_T = CW_U + 4608  , CW_SIZE = CW_T + 4096  ;

DEV void conv_unit(LAS char* lds, const u16* __restrict__ q, const u16* __restrict__ kk, const u16* __restrict__ vv,
                   const u16* __restrict__ shk, const u16* __restrict__ ssmk, const u16* __restrict__ ssmD,
                   const u16* __restrict__ wd, const u16* __restrict__ gw, char* __restrict__ ws, int cu) {
  const int tid = threadIdx.x, lane = tid & 63;
  const int wid = __builtin_amdgcn_readfirstlane(tid >> 6);
  const int fr = lane & 15, fq = lane >> 4;
  LAS char* wl = lds + wid * CW_SIZE;
  char* kvT = ws + WS_KVT;
  {
    const int cq = tid & 3, tb = tid >> 2;
    const int ch0 = cu * 32 + cq * 8;
    float wt[8][4];
    {
      const u32x4* sp = (const u32x4*)(shk + ch0 * 4);
#pragma unroll
      for (int c2 = 0; c2 < 4; ++c2) {
        u32x4 t = sp[c2];
        wt[c2 * 2][0] = bflo(t[0]); wt[c2 * 2][1] = bfhi(t[0]); wt[c2 * 2][2] = bflo(t[1]); wt[c2 * 2][3] = bfhi(t[1]);
        wt[c2 * 2 + 1][0] = bflo(t[2]); wt[c2 * 2 + 1][1] = bfhi(t[2]); wt[c2 * 2 + 1][2] = bflo(t[3]); wt[c2 * 2 + 1][3] = bfhi(t[3]);
      }
    }
#pragma unroll
    for (int hf = 0; hf < 2; ++hf) {
      const int t0 = tb * 16 + hf * 8;
      u32x4 kr[11], vr[8];
#pragma unroll
      for (int j = 0; j < 11; ++j) {
        const int t = t0 - 3 + j;
        const int tc = t < 0 ? 0 : t;
        u32x4 val = *(const u32x4*)(kk + (size_t)tc * D_ + ch0);
        if (t < 0) val = (u32x4){0u, 0u, 0u, 0u};
        kr[j] = val;
      }
#pragma unroll
      for (int j = 0; j < 8; ++j) vr[j] = *(const u32x4*)(vv + (size_t)(t0 + j) * D_ + ch0);
#pragma unroll
      for (int c = 0; c < 8; ++c) {
        float kf[11];
#pragma unroll
        for (int j = 0; j < 11; ++j) kf[j] = (c & 1) ? bfhi(kr[j][c >> 1]) : bflo(kr[j][c >> 1]);
        float o[8];
#pragma unroll
        for (int j = 0; j < 8; ++j) {
          const float ksft = kf[j] * wt[c][3] + kf[j + 1] * wt[c][2] + kf[j + 2] * wt[c][1] + kf[j + 3] * wt[c][0];
          const float vf = (c & 1) ? bfhi(vr[j][c >> 1]) : bflo(vr[j][c >> 1]);
          o[j] = ksft * vf;
        }
        u32x4 ck = (u32x4){pack2(o[7], o[6]), pack2(o[5], o[4]), pack2(o[3], o[2]), pack2(o[1], o[0])};
        *(u32x4*)(kvT + (size_t)(ch0 + c) * 4096 + (t0 >> 3) * 16) = ck;
      }
    }
  }
  {
    u32x4 gwv[2];
    gwv[0] = *(const u32x4*)(gw + lane * 8); gwv[1] = *(const u32x4*)(gw + 512 + lane * 8);
#pragma unroll
    for (int r = 0; r < 8; ++r) {
      const int row = cu * 64 + wid * 8 + r;
      u32x4 a0 = *(const u32x4*)(q + (size_t)row * D_ + lane * 8), a1 = *(const u32x4*)(q + (size_t)row * D_ + 512 + lane * 8);
      float s = 0.f;
#pragma unroll
      for (int e = 0; e < 4; ++e) {
        s += rbf(bflo(a0[e]) * bflo(gwv[0][e])) + rbf(bfhi(a0[e]) * bfhi(gwv[0][e]));
        s += rbf(bflo(a1[e]) * bflo(gwv[1][e])) + rbf(bfhi(a1[e]) * bfhi(gwv[1][e]));
      }
#pragma unroll
      for (int o = 32; o >= 1; o >>= 1) s += __shfl_xor(s, o);
      if (lane == 0) { s = rbf(s); ((float*)(ws + WS_G))[row] = rbf(1.0f / (1.0f + __expf(-s))); }
    }
  }
  {
    const int q4 = (lane & 15) >> 2, p = lane & 3;
    for (int b = 0; b < 7; ++b) {
      const int blk = (cu * 8 + wid) * 7 + b;
      const int ktile = blk >> 4, ct = blk & 15;
      const char* src = (const char*)wd + (size_t)(ktile * 32 + (lane >> 3)) * 2048 + ct * 128 + (lane & 7) * 16;
#pragma unroll
      for (int i = 0; i < 4; ++i) glds16(src + (size_t)i * 8 * 2048, wl + CW_T + i * 1024);
      WAIT_V(0);
      s16x8 fr4[4];
#pragma unroll
      for (int jj = 0; jj < 4; ++jj) {
        LAS char* p0 = wl + CW_T + (4 * fq + q4) * 128 + (16 * p + 4 * jj) * 2;
        fr4[jj] = tr_read8(p0, p0 + 16 * 128);
      }
#pragma unroll
      for (int jj = 0; jj < 4; ++jj) *(s16x8*)(ws + WS_WDF + ((size_t)(blk * 4 + jj) * 64 + lane) * 16) = fr4[jj];
      WAIT_L(0);
    }
  }
  if (lane < 32) *(LAS u32x4*)(wl + CW_U + lane * 16) = (u32x4){0u, 0u, 0u, 0u};
  __syncthreads();

  const int o_ = 1 + fr + 8 * fq;
  LAS char* pbase = wl + CW_H + (o_ & 3) * CW_CS + 2 * (o_ & ~3);
  LAS char* qbase = wl + CW_U + 16 * (33 + 2 * fr - fq) - 448;
  for (int rnd = 0; rnd < 4; ++rnd) {
    const int ch = cu * 32 + rnd * 8 + wid;
    const u16* hrow = ssmk + (size_t)ch * S_;
    {
      const char* src = kvT + (size_t)ch * 4096 + lane * 16;
#pragma unroll
      for (int i = 0; i < 4; ++i) glds16(src + i * 1024, wl + CW_U + 512 + i * 1024);
    }
    f32x4 acc[8];
#pragma unroll
    for (int g = 0; g < 8; ++g) acc[g] = (f32x4){0.f, 0.f, 0.f, 0.f};
#pragma unroll
    for (int half = 0; half < 2; ++half) {
#pragma unroll
      for (int ps = 0; ps < 3; ++ps) {
        const int sl = ps * 64 + lane;
        if (ps < 2 || lane < 2) {
          const int sg = half * 128 + sl;
          const int hi0 = 8 * sg - 16;
          u32x4 a = (u32x4){0u, 0u, 0u, 0u}; u32x2 b = (u32x2){0u, 0u};
          if (hi0 >= 0) a = *(const u32x4*)(hrow + hi0);
          if (hi0 + 8 >= 0 && hi0 + 8 < S_) b = *(const u32x2*)(hrow + hi0 + 8);
          const unsigned a01 = __builtin_amdgcn_alignbit(a[1], a[0], 16), a12 = __builtin_amdgcn_alignbit(a[2], a[1], 16),
                         a23 = __builtin_amdgcn_alignbit(a[3], a[2], 16), a34 = __builtin_amdgcn_alignbit(b[0], a[3], 16),
                         a45 = __builtin_amdgcn_alignbit(b[1], b[0], 16);
          LAS char* dst = wl + CW_H + sl * 16;
          *(LAS u32x4*)(dst) = a;
          *(LAS u32x4*)(dst + CW_CS) = (u32x4){a01, a12, a23, a34};
          *(LAS u32x4*)(dst + 2 * CW_CS) = (u32x4){a[1], a[2], a[3], b[0]};
          *(LAS u32x4*)(dst + 3 * CW_CS) = (u32x4){a12, a23, a34, a45};
        }
      }
      WAIT_V(0); WAIT_L(0);
      for (int r = 0; r < 8; ++r) {
        s16x8 P[4], Q[8];
#pragma unroll
        for (int a = 0; a < 4; ++a) {
          volatile LAS s16x4* pp = (volatile LAS s16x4*)(pbase + r * 64 + a * 512);
          s16x4 lo = pp[0], hi = pp[1];
          P[a] = __builtin_shufflevector(lo, hi, 0, 1, 2, 3, 4, 5, 6, 7);
        }
#pragma unroll
        for (int b = 0; b < (half ? 4 : 8); ++b) Q[b] = *(LAS s16x8*)(qbase + b * 512 + (7 - r) * 64);
#pragma unroll
        for (int a = 0; a < 4; ++a)
#pragma unroll
          for (int b = 0; b < 8; ++b)
            if (half * 4 + a + b < 8) acc[half * 4 + a + b] = mfma16(P[a], Q[b], acc[half * 4 + a + b]);
      }
      WAIT_L(0);
    }
    const float dv = bflo((unsigned)ssmD[ch]);
#pragma unroll
    for (int g = 0; g < 8; ++g) {
      const int al = 32 * g + 2 * fr + (fq >> 1);
      const u32x2 kvp = *(LAS u32x2*)(wl + CW_U + 512 + al * 16 + ((fq & 1) ? 0 : 8));
      const float k3 = bflo(kvp[0]), k2 = bfhi(kvp[0]), k1 = bflo(kvp[1]), k0 = bfhi(kvp[1]);
      const float z0 = rbf(acc[g][0]) + rbf(k0 * dv), z1 = rbf(acc[g][1]) + rbf(k1 * dv), z2 = rbf(acc[g][2]) + rbf(k2 * dv), z3 = rbf(acc[g][3]) + rbf(k3 * dv);
      *(u32x2*)(ws + WS_ZT + (size_t)ch * 4096 + (256 * g + 16 * fr + 4 * fq) * 2) = (u32x2){pack2(z0, z1), pack2(z2, z3)};
    }
    WAIT_L(0);
  }
}

__global__ __launch_bounds__(512, 2) void k1_kernel(const u16* __restrict__ q, const u16* __restrict__ kk, const u16* __restrict__ vv,
                                                    const u16* __restrict__ shk, const u16* __restrict__ ssmk, const u16* __restrict__ ssmD,
                                                    const u16* __restrict__ wg, const u16* __restrict__ wu, const u16* __restrict__ wd,
                                                    const u16* __restrict__ gw, char* __restrict__ ws) {
  extern __shared__ __attribute__((aligned(16))) char shm[];
  LAS char* lds = (LAS char*)shm;
  const int b = blockIdx.x, xcd = b & 7, j = b >> 3;
  if (j < 4) {
    conv_unit(lds, q, kk, vv, shk, ssmk, ssmD, wd, gw, ws, xcd * 4 + j);
  } else {
    const int jj = j - 4;
    const int pm = (xcd & 1) * 4 + (jj & 3), pn = (xcd >> 1) * 7 + (jj >> 2);
    gemm1_tile(lds, q, wg, wu, ws + WS_HF, pm, pn);
  }
}

constexpr int PD = 6;
__global__ __launch_bounds__(256, 1) void k2_kernel(const u16* __restrict__ q, const char* __restrict__ ws, u16* __restrict__ out) {
  extern __shared__ __attribute__((aligned(16))) char shm[];
  LAS char* lds = (LAS char*)shm;
  const int tid = threadIdx.x, lane = tid & 63;
  const int wid = __builtin_amdgcn_readfirstlane(tid >> 6);
  const int fr = lane & 15, fq = lane >> 4;
  const int b = blockIdx.x, xcd = b & 7, j = b >> 3;
  const int rt = (xcd >> 1) * 4 + (j >> 3), ct = (xcd & 1) * 8 + (j & 7);
  const char* ha = ws + WS_HF + (size_t)(wid * 128 + rt * 8) * 1024;
  const char* wb = ws + WS_WDF + (size_t)(wid * 64 + ct * 4) * 1024;
  const unsigned lo16 = lane * 16;
  char* stamp_base = (char*)ws + (64u << 20) + 256 * 128;
  STAMP_AT(0);
  f32x4 acc[8][4];
#pragma unroll
  for (int i = 0; i < 8; ++i)
#pragma unroll
    for (int n = 0; n < 4; ++n) acc[i][n] = (f32x4){0.f, 0.f, 0.f, 0.f};
  s16x8 A[PD][8], B[PD][4];
  auto load_step = [&](int s, int slot) {
    const char* pa = ha + (size_t)s * (4 * 128 * 1024);
    const char* pb = wb + (size_t)s * (4 * 64 * 1024);
#pragma unroll
    for (int n = 0; n < 4; ++n) B[slot][n] = *(const s16x8*)(pb + n * 1024 + lo16);
#pragma unroll
    for (int i = 0; i < 8; ++i) A[slot][i] = *(const s16x8*)(pa + i * 1024 + lo16);
  };
#pragma unroll
  for (int s = 0; s < PD - 1; ++s) load_step(s, s);
#pragma unroll
  for (int s = 0; s < 28; ++s) {
    if (s + PD - 1 < 28) load_step(s + PD - 1, (s + PD - 1) % PD);
    SCHED();
#pragma unroll
    for (int i = 0; i < 8; ++i)
#pragma unroll
      for (int n = 0; n < 4; ++n) acc[i][n] = mfma16(B[s % PD][n], A[s % PD][i], acc[i][n]);
    SCHED();
  }
  STAMP_AT(4);
  {
    const char* src = ws + WS_ZT + (size_t)(ct * 64 + wid * 16 + (lane >> 4)) * 4096 + rt * 256 + (lane & 15) * 16;
#pragma unroll
    for (int i = 0; i < 4; ++i) glds16(src + (size_t)i * 4 * 4096, lds + 98304 + wid * 4096 + i * 1024);
  }
#pragma unroll
  for (int o = 0; o < 4; ++o) {
    if (o != wid) {
      const int slot = (wid < o) ? wid : wid - 1;
#pragma unroll
      for (int i2 = 0; i2 < 2; ++i2)
#pragma unroll
        for (int n = 0; n < 4; ++n)
          *(LAS f32x4*)(lds + ((o * 3 + slot) * 8 + i2 * 4 + n) * 1024 + lo16) = acc[o * 2 + i2][n];
    }
  }
  f32x4 fin[2][4];
#pragma unroll
  for (int o = 0; o < 4; ++o)
    if (o == wid) {
#pragma unroll
      for (int i2 = 0; i2 < 2; ++i2)
#pragma unroll
        for (int n = 0; n < 4; ++n) fin[i2][n] = acc[o * 2 + i2][n];
    }
  u32x4 qv[2][2]; float gv[2];
#pragma unroll
  for (int i2 = 0; i2 < 2; ++i2) {
    const int row = rt * 128 + (wid * 2 + i2) * 16 + fr;
    const u16* qp = q + (size_t)row * D_ + ct * 64 + fq * 16;
    qv[i2][0] = *(const u32x4*)qp; qv[i2][1] = *(const u32x4*)(qp + 8);
    gv[i2] = ((const float*)(ws + WS_G))[row];
  }
  WAIT_V(0); WAIT_L(0);
  STAMP_AT(5);
  __syncthreads();
  STAMP_AT(6);
#pragma unroll
  for (int s = 0; s < 3; ++s)
#pragma unroll
    for (int i2 = 0; i2 < 2; ++i2)
#pragma unroll
      for (int n = 0; n < 4; ++n) fin[i2][n] += *(LAS f32x4*)(lds + ((wid * 3 + s) * 8 + i2 * 4 + n) * 1024 + lo16);
  const int q4 = (lane & 15) >> 2, p = lane & 3;
#pragma unroll
  for (int i2 = 0; i2 < 2; ++i2) {
    const int row = rt * 128 + (wid * 2 + i2) * 16 + fr;
    const float g = gv[i2], og = rbf(1.0f - g);
    unsigned ow[8];
#pragma unroll
    for (int n = 0; n < 4; ++n) {
      LAS char* zp = lds + 98304 + (16 * fq + 4 * n + q4) * 256 + ((wid * 2 + i2) * 16 + 4 * p) * 2;
      const s16x4 zz = __builtin_amdgcn_ds_read_tr16_b64_v4i16((LAS s16x4*)zp);
      const u32x2 zu = __builtin_bit_cast(u32x2, zz);
      const unsigned q01 = qv[i2][n >> 1][(n & 1) * 2], q23 = qv[i2][n >> 1][(n & 1) * 2 + 1];
      float r4[4];
      const float zf[4] = {bflo(zu[0]), bfhi(zu[0]), bflo(zu[1]), bfhi(zu[1])};
      const float qf[4] = {bflo(q01), bfhi(q01), bflo(q23), bfhi(q23)};
#pragma unroll
      for (int r = 0; r < 4; ++r) {
        const float a = rbf(qf[r] * zf[r]);
        r4[r] = rbf(g * a) + rbf(og * rbf(fin[i2][n][r]));
      }
      ow[n * 2] = pack2(r4[0], r4[1]); ow[n * 2 + 1] = pack2(r4[2], r4[3]);
    }
    u16* op = out + (size_t)row * D_ + ct * 64 + fq * 16;
    *(u32x4*)op = (u32x4){ow[0], ow[1], ow[2], ow[3]};
    *(u32x4*)(op + 8) = (u32x4){ow[4], ow[5], ow[6], ow[7]};
  }
  WAIT_V(0);
  STAMP_AT(7);
}

extern "C" void kernel_launch(void* const* d_in, const int* in_sizes, int n_in, void* d_out, int out_size, void* d_ws, size_t ws_size,
                              hipStream_t stream) {
  const u16* q = (const u16*)d_in[0]; const u16* kk = (const u16*)d_in[1]; const u16* vv = (const u16*)d_in[2];
  const u16* shk = (const u16*)d_in[3]; const u16* ssmk = (const u16*)d_in[4]; const u16* ssmD = (const u16*)d_in[5];
  const u16* wg = (const u16*)d_in[7]; const u16* wu = (const u16*)d_in[8]; const u16* wd = (const u16*)d_in[9];
  const u16* gw = (const u16*)d_in[10];
  constexpr int LDS1 = 8 * CW_SIZE > 131072 ? 8 * CW_SIZE : 131072;
  constexpr int LDS2 = 98304 + 16384;
  hipFuncSetAttribute((const void*)k1_kernel, hipFuncAttributeMaxDynamicSharedMemorySize, LDS1);
  hipFuncSetAttribute((const void*)k2_kernel, hipFuncAttributeMaxDynamicSharedMemorySize, LDS2);
  k1_kernel<<<256, 512, LDS1, stream>>>(q, kk, vv, shk, ssmk, ssmD, wg, wu, wd, gw, (char*)d_ws);
  k2_kernel<<<256, 256, LDS2, stream>>>(q, (const char*)d_ws, (u16*)d_out);
}
```

```cpp
#include <hip/hip_runtime.h>
#include <hip/hip_bf16.h>

typedef unsigned short u16;
typedef short s16x4 __attribute__((ext_vector_type(4)));
typedef short s16x8 __attribute__((ext_vector_type(8)));
typedef __bf16 bfx2 __attribute__((ext_vector_type(2)));
typedef __bf16 bfx8 __attribute__((ext_vector_type(8)));
typedef float f32x4 __attribute__((ext_vector_type(4)));
typedef float f32x2 __attribute__((ext_vector_type(2)));
typedef unsigned u32x4 __attribute__((ext_vector_type(4)));
typedef unsigned u32x2 __attribute__((ext_vector_type(2)));
#define LAS __attribute__((address_space(3)))
#define DEV __device__ __forceinline__

#define PF_FIRST 2
#define PF_AHEAD 8
#define PF_PACE 2200
#define PF_T0 5000
constexpr int S_ = 2048, D_ = 1024, F_ = 3584;
constexpr size_t WS_HF = 0;
constexpr size_t WS_WDF = 16u << 20;
constexpr size_t WS_KVT = 24u << 20;
constexpr size_t WS_ZT = 28u << 20;
constexpr size_t WS_G = 32u << 20;

DEV float bflo(unsigned w) { return __uint_as_float(w << 16); }
DEV float bfhi(unsigned w) { return __uint_as_float(w & 0xffff0000u); }
DEV unsigned pack2(float lo, float hi) { bfx2 p; p[0] = (__bf16)lo; p[1] = (__bf16)hi; return __builtin_bit_cast(unsigned, p); }
DEV float rbf(float f) { return (float)(__bf16)f; }
DEV f32x4 mfma16(s16x8 a, s16x8 b, f32x4 c) {
  return __builtin_amdgcn_mfma_f32_16x16x32_bf16(__builtin_bit_cast(bfx8, a), __builtin_bit_cast(bfx8, b), c, 0, 0, 0);
}
DEV void mfma16a(f32x4& c, s16x8 a, s16x8 b) {
  asm volatile("v_mfma_f32_16x16x32_bf16 %0, %1, %2, %0" : "+v"(c) : "v"(a), "v"(b));
}
DEV s16x8 tr_read8(LAS char* p0, LAS char* p1) {
  s16x4 lo = __builtin_amdgcn_ds_read_tr16_b64_v4i16((LAS s16x4*)p0);
  s16x4 hi = __builtin_amdgcn_ds_read_tr16_b64_v4i16((LAS s16x4*)p1);
  return __builtin_shufflevector(lo, hi, 0, 1, 2, 3, 4, 5, 6, 7);
}
DEV void glds16(const char* g, LAS char* l) {
  __builtin_amdgcn_global_load_lds((const __attribute__((address_space(1))) unsigned*)g, (LAS unsigned*)l, 16, 0, 0);
}
typedef __amdgpu_buffer_rsrc_t rsrc_t;
DEV rsrc_t mk_rsrc(const void* p, unsigned bytes) { return __builtin_amdgcn_make_buffer_rsrc((void*)p, 0, bytes, 0x00020000); }
DEV void blds16(rsrc_t r, LAS char* l, unsigned voff, unsigned soff) {
  __builtin_amdgcn_raw_ptr_buffer_load_lds(r, (LAS void*)l, 16, voff, soff, 0, 0);
}
#define STAMP_AT(i)
#define WAIT_V(n) asm volatile("s_waitcnt vmcnt(" #n ")" ::: "memory")
#define WAIT_L(n) asm volatile("s_waitcnt lgkmcnt(" #n ")" ::: "memory")
#define BAR() __builtin_amdgcn_s_barrier()
#define SCHED() __builtin_amdgcn_sched_barrier(0)

DEV void gemm1_tile(LAS char* lds, const u16* __restrict__ x, const u16* __restrict__ wg, const u16* __restrict__ wu,
                    char* __restrict__ hF, int pm, int pn) {
  const int tid = threadIdx.x, lane = tid & 63;
  const int wid = __builtin_amdgcn_readfirstlane(tid >> 6);
  const int wr = wid >> 1, wc = wid & 1;
  char* stamp_base = hF + (64u << 20);
  STAMP_AT(0);
  const int fr = lane & 15, fq = lane >> 4;
  unsigned va[2], vb[2];
#pragma unroll
  for (int e = 0; e < 2; ++e) {
    int rl = lane >> 3, p = lane & 7;
    int sw = (e << 2) | (lane >> 4);
    va[e] = (unsigned)(rl * 2048 + ((p ^ sw) * 16));
    int kl = lane >> 4, pb = lane & 15;
    int swb = (kl | (e << 2)) << 1;
    vb[e] = (unsigned)(kl * (F_ * 2) + ((pb ^ swb) * 16));
  }
  const rsrc_t rx = mk_rsrc(x, S_ * D_ * 2);
  const rsrc_t rw = mk_rsrc((wid < 4) ? wg : wu, D_ * F_ * 2);
  const unsigned xa_base = (unsigned)(pm * 256 + wid * 32) * 2048u;
  const unsigned wb_base = (unsigned)((wid & 3) * 16) * (F_ * 2) + pn * 256;
  LAS char* ldsA = lds + wid * 4096;
  LAS char* ldsB = lds + 65536 + wid * 4096;
  auto dmaA = [&](int kt, int st, int i) { blds16(rx, ldsA + st * 32768 + i * 1024, va[i & 1], xa_base + i * (8 * 2048) + kt * 128); };
  auto dmaB = [&](int kt, int st, int i) { blds16(rw, ldsB + st * 32768 + i * 1024, vb[i >> 1], wb_base + (kt * 64 + i * 4) * (F_ * 2)); };
  LAS char* bx[2];
#pragma unroll
  for (int ks = 0; ks < 2; ++ks) bx[ks] = lds + (wr * 64 + fr) * 128 + (((ks * 4 + fq) ^ (fr >> 1)) * 16);
  LAS char* bw[4];
  {
    int q = (lane & 15) >> 2, p = lane & 3;
    int sw = (q | ((fq & 1) << 2)) << 1;
#pragma unroll
    for (int j = 0; j < 4; ++j) bw[j] = lds + 65536 + (8 * fq + q) * 256 + (((wc * 8 + j * 2 + (p >> 1)) ^ sw) * 16) + (p & 1) * 8;
  }
  auto ldx = [&](int st, int ks, int i) -> s16x8 { return *(LAS s16x8*)(bx[ks] + st * 32768 + i * 2048); };
  auto ldw = [&](int st, int ks, int nf) -> s16x8 {
    LAS char* p = bw[nf & 3] + st * 32768 + (nf >> 2) * 16384 + ks * 8192;
    return tr_read8(p, p + 1024);
  };
  f32x4 acc[4][8];
#pragma unroll
  for (int i = 0; i < 4; ++i)
#pragma unroll
    for (int n = 0; n < 8; ++n) acc[i][n] = (f32x4){0.f, 0.f, 0.f, 0.f};
  s16x8 xa[4], xb[4], w[8];
#pragma unroll
  for (int i = 0; i < 4; ++i) { dmaA(0, 0, i); dmaB(0, 0, i); }
#pragma unroll
  for (int i = 0; i < 4; ++i) { dmaA(1, 1, i); dmaB(1, 1, i); }
  WAIT_V(8); BAR(); SCHED();
  STAMP_AT(1);
#pragma unroll
  for (int i = 0; i < 4; ++i) xa[i] = ldx(0, 0, i);
#pragma unroll
  for (int n = 0; n < 8; ++n) w[n] = ldw(0, 0, n);
  SCHED();

  auto tile_body = [&](const int st, const int kt, const bool pre1, const bool pre2) {
#pragma unroll
    for (int n = 0; n < 8; ++n) {
#pragma unroll
      for (int i = 0; i < 4; ++i) mfma16a(acc[i][n], w[n], xa[i]);
      w[n] = ldw(st, 1, n);
      if (n < 4) xb[n] = ldx(st, 1, n);
      SCHED();
    }
#pragma unroll
    for (int n = 0; n < 2; ++n) {
#pragma unroll
      for (int i = 0; i < 4; ++i) mfma16a(acc[i][n], w[n], xb[i]);
    }
    SCHED();
    if (pre1) {
      WAIT_L(0);
      WAIT_V(0);
      BAR();
      SCHED();
      w[0] = ldw(st ^ 1, 0, 0); w[1] = ldw(st ^ 1, 0, 1);
      if (pre2) { dmaA(kt + 2, st, 0); dmaB(kt + 2, st, 0); }
      SCHED();
    }
#pragma unroll
    for (int n = 2; n < 8; ++n) {
#pragma unroll
      for (int i = 0; i < 4; ++i) mfma16a(acc[i][n], w[n], xb[i]);
      if (pre1) {
        w[n] = ldw(st ^ 1, 0, n);
        if (n - 2 < 4) xa[n - 2] = ldx(st ^ 1, 0, n - 2);
        if (pre2 && n < 5) { dmaA(kt + 2, st, n - 1); dmaB(kt + 2, st, n - 1); }
      }
      SCHED();
    }
  };
  for (int kt = 0; kt < 14; kt += 2) {
    tile_body(0, kt, true, true);
    tile_body(1, kt + 1, true, true);
  }
  STAMP_AT(2);
  tile_body(0, 14, true, false);
  tile_body(1, 15, false, false);
  STAMP_AT(3);

  asm volatile("s_nop 7\n\ts_nop 7" ::: "memory");
  SCHED();
  const float C1 = -2.302208198f, C2 = -2.302208198f * 0.044715f;
#pragma unroll
  for (int i = 0; i < 4; ++i) {
    const int mtile = pm * 16 + wr * 4 + i;
#pragma unroll
    for (int k2 = 0; k2 < 2; ++k2) {
      unsigned pk[4];
#pragma unroll
      for (int jj = 0; jj < 2; ++jj) {
        const f32x4 gt = acc[i][k2 * 2 + jj], up = acc[i][4 + k2 * 2 + jj];
        const f32x4 wv = gt * (gt * gt * C2 + C1);
        f32x4 ev;
#pragma unroll
        for (int r = 0; r < 4; ++r) ev[r] = __builtin_amdgcn_exp2f(wv[r]);
        const f32x4 dv = ev + 1.0f;
        f32x4 rv;
#pragma unroll
        for (int r = 0; r < 4; ++r) rv[r] = __builtin_amdgcn_rcpf(dv[r]);
        const f32x4 hv = gt * up * rv;
        pk[jj * 2] = pack2(hv[0], hv[1]); pk[jj * 2 + 1] = pack2(hv[2], hv[3]);
      }
      const int ktile = pn * 4 + wc * 2 + k2;
      *(u32x4*)(hF + ((size_t)(ktile * 128 + mtile) * 64 + lane) * 16) = (u32x4){pk[0], pk[1], pk[2], pk[3]};
    }
  }
  STAMP_AT(4);
  WAIT_V(0);
  STAMP_AT(7);
}

constexpr int CW_H = 0, CW_CS = 2112, CW_U = 4 * 2112  , CW_T = CW_U + 4608  , CW_SIZE = CW_T + 4096  ;

DEV void conv_unit(LAS char* lds, const u16* __restrict__ q, const u16* __restrict__ kk, const u16* __restrict__ vv,
                   const u16* __restrict__ shk, const u16* __restrict__ ssmk, const u16* __restrict__ ssmD,
                   const u16* __restrict__ wd, const u16* __restrict__ gw, char* __restrict__ ws, int cu) {
  const int tid = threadIdx.x, lane = tid & 63;
  const int wid = __builtin_amdgcn_readfirstlane(tid >> 6);
  const int fr = lane & 15, fq = lane >> 4;
  LAS char* wl = lds + wid * CW_SIZE;
  char* kvT = ws + WS_KVT;
  {
    const int cq = tid & 3, tb = tid >> 2;
    const int ch0 = cu * 32 + cq * 8;
    float wt[8][4];
    {
      const u32x4* sp = (const u32x4*)(shk + ch0 * 4);
#pragma unroll
      for (int c2 = 0; c2 < 4; ++c2) {
        u32x4 t = sp[c2];
        wt[c2 * 2][0] = bflo(t[0]); wt[c2 * 2][1] = bfhi(t[0]); wt[c2 * 2][2] = bflo(t[1]); wt[c2 * 2][3] = bfhi(t[1]);
        wt[c2 * 2 + 1][0] = bflo(t[2]); wt[c2 * 2 + 1][1] = bfhi(t[2]); wt[c2 * 2 + 1][2] = bflo(t[3]); wt[c2 * 2 + 1][3] = bfhi(t[3]);
      }
    }
#pragma unroll
    for (int hf = 0; hf < 2; ++hf) {
      const int t0 = tb * 16 + hf * 8;
      u32x4 kr[11], vr[8];
#pragma unroll
      for (int j = 0; j < 11; ++j) {
        const int t = t0 - 3 + j;
        const int tc = t < 0 ? 0 : t;
        u32x4 val = *(const u32x4*)(kk + (size_t)tc * D_ + ch0);
        if (t < 0) val = (u32x4){0u, 0u, 0u, 0u};
        kr[j] = val;
      }
#pragma unroll
      for (int j = 0; j < 8; ++j) vr[j] = *(const u32x4*)(vv + (size_t)(t0 + j) * D_ + ch0);
#pragma unroll
      for (int c = 0; c < 8; ++c) {
        float kf[11];
#pragma unroll
        for (int j = 0; j < 11; ++j) kf[j] = (c & 1) ? bfhi(kr[j][c >> 1]) : bflo(kr[j][c >> 1]);
        float o[8];
#pragma unroll
        for (int j = 0; j < 8; ++j) {
          const float ksft = kf[j] * wt[c][3] + kf[j + 1] * wt[c][2] + kf[j + 2] * wt[c][1] + kf[j + 3] * wt[c][0];
          const float vf = (c & 1) ? bfhi(vr[j][c >> 1]) : bflo(vr[j][c >> 1]);
          o[j] = ksft * vf;
        }
        u32x4 ck = (u32x4){pack2(o[7], o[6]), pack2(o[5], o[4]), pack2(o[3], o[2]), pack2(o[1], o[0])};
        *(u32x4*)(kvT + (size_t)(ch0 + c) * 4096 + (t0 >> 3) * 16) = ck;
      }
    }
  }
  {
    u32x4 gwv[2];
    gwv[0] = *(const u32x4*)(gw + lane * 8); gwv[1] = *(const u32x4*)(gw + 512 + lane * 8);
#pragma unroll
    for (int r = 0; r < 8; ++r) {
      const int row = cu * 64 + wid * 8 + r;
      u32x4 a0 = *(const u32x4*)(q + (size_t)row * D_ + lane * 8), a1 = *(const u32x4*)(q + (size_t)row * D_ + 512 + lane * 8);
      float s = 0.f;
#pragma unroll
      for (int e = 0; e < 4; ++e) {
        s += rbf(bflo(a0[e]) * bflo(gwv[0][e])) + rbf(bfhi(a0[e]) * bfhi(gwv[0][e]));
        s += rbf(bflo(a1[e]) * bflo(gwv[1][e])) + rbf(bfhi(a1[e]) * bfhi(gwv[1][e]));
      }
#pragma unroll
      for (int o = 32; o >= 1; o >>= 1) s += __shfl_xor(s, o);
      if (lane == 0) { s = rbf(s); ((float*)(ws + WS_G))[row] = rbf(1.0f / (1.0f + __expf(-s))); }
    }
  }
  {
    const int q4 = (lane & 15) >> 2, p = lane & 3;
    for (int b = 0; b < 7; ++b) {
      const int blk = (cu * 8 + wid) * 7 + b;
      const int ktile = blk >> 4, ct = blk & 15;
      const char* src = (const char*)wd + (size_t)(ktile * 32 + (lane >> 3)) * 2048 + ct * 128 + (lane & 7) * 16;
#pragma unroll
      for (int i = 0; i < 4; ++i) glds16(src + (size_t)i * 8 * 2048, wl + CW_T + i * 1024);
      WAIT_V(0);
      s16x8 fr4[4];
#pragma unroll
      for (int jj = 0; jj < 4; ++jj) {
        LAS char* p0 = wl + CW_T + (4 * fq + q4) * 128 + (16 * p + 4 * jj) * 2;
        fr4[jj] = tr_read8(p0, p0 + 16 * 128);
      }
#pragma unroll
      for (int jj = 0; jj < 4; ++jj) *(s16x8*)(ws + WS_WDF + ((size_t)(blk * 4 + jj) * 64 + lane) * 16) = fr4[jj];
      WAIT_L(0);
    }
  }
  if (lane < 32) *(LAS u32x4*)(wl + CW_U + lane * 16) = (u32x4){0u, 0u, 0u, 0u};
  __syncthreads();

  const int o_ = 1 + fr + 8 * fq;
  LAS char* pbase = wl + CW_H + (o_ & 3) * CW_CS + 2 * (o_ & ~3);
  LAS char* qbase = wl + CW_U + 16 * (33 + 2 * fr - fq) - 448;
  for (int rnd = 0; rnd < 4; ++rnd) {
    const int ch = cu * 32 + rnd * 8 + wid;
    const u16* hrow = ssmk + (size_t)ch * S_;
    {
      const char* src = kvT + (size_t)ch * 4096 + lane * 16;
#pragma unroll
      for (int i = 0; i < 4; ++i) glds16(src + i * 1024, wl + CW_U + 512 + i * 1024);
    }
    f32x4 acc[8];
#pragma unroll
    for (int g = 0; g < 8; ++g) acc[g] = (f32x4){0.f, 0.f, 0.f, 0.f};
#pragma unroll
    for (int half = 0; half < 2; ++half) {
#pragma unroll
      for (int ps = 0; ps < 3; ++ps) {
        const int sl = ps * 64 + lane;
        if (ps < 2 || lane < 2) {
          const int sg = half * 128 + sl;
          const int hi0 = 8 * sg - 16;
          u32x4 a = (u32x4){0u, 0u, 0u, 0u}; u32x2 b = (u32x2){0u, 0u};
          if (hi0 >= 0) a = *(const u32x4*)(hrow + hi0);
          if (hi0 + 8 >= 0 && hi0 + 8 < S_) b = *(const u32x2*)(hrow + hi0 + 8);
          const unsigned a01 = __builtin_amdgcn_alignbit(a[1], a[0], 16), a12 = __builtin_amdgcn_alignbit(a[2], a[1], 16),
                         a23 = __builtin_amdgcn_alignbit(a[3], a[2], 16), a34 = __builtin_amdgcn_alignbit(b[0], a[3], 16),
                         a45 = __builtin_amdgcn_alignbit(b[1], b[0], 16);
          LAS char* dst = wl + CW_H + sl * 16;
          *(LAS u32x4*)(dst) = a;
          *(LAS u32x4*)(dst + CW_CS) = (u32x4){a01, a12, a23, a34};
          *(LAS u32x4*)(dst + 2 * CW_CS) = (u32x4){a[1], a[2], a[3], b[0]};
          *(LAS u32x4*)(dst + 3 * CW_CS) = (u32x4){a12, a23, a34, a45};
        }
      }
      WAIT_V(0); WAIT_L(0);
      for (int r = 0; r < 8; ++r) {
        s16x8 P[4], Q[8];
#pragma unroll
        for (int a = 0; a < 4; ++a) {
          volatile LAS s16x4* pp = (volatile LAS s16x4*)(pbase + r * 64 + a * 512);
          s16x4 lo = pp[0], hi = pp[1];
          P[a] = __builtin_shufflevector(lo, hi, 0, 1, 2, 3, 4, 5, 6, 7);
        }
#pragma unroll
        for (int b = 0; b < (half ? 4 : 8); ++b) Q[b] = *(LAS s16x8*)(qbase + b * 512 + (7 - r) * 64);
#pragma unroll
        for (int a = 0; a < 4; ++a)
#pragma unroll
          for (int b = 0; b < 8; ++b)
            if (half * 4 + a + b < 8) acc[half * 4 + a + b] = mfma16(P[a], Q[b], acc[half * 4 + a + b]);
      }
      WAIT_L(0);
    }
    const float dv = bflo((unsigned)ssmD[ch]);
#pragma unroll
    for (int g = 0; g < 8; ++g) {
      const int al = 32 * g + 2 * fr + (fq >> 1);
      const u32x2 kvp = *(LAS u32x2*)(wl + CW_U + 512 + al * 16 + ((fq & 1) ? 0 : 8));
      const float k3 = bflo(kvp[0]), k2 = bfhi(kvp[0]), k1 = bflo(kvp[1]), k0 = bfhi(kvp[1]);
      const float z0 = rbf(acc[g][0]) + rbf(k0 * dv), z1 = rbf(acc[g][1]) + rbf(k1 * dv), z2 = rbf(acc[g][2]) + rbf(k2 * dv), z3 = rbf(acc[g][3]) + rbf(k3 * dv);
      *(u32x2*)(ws + WS_ZT + (size_t)ch * 4096 + (256 * g + 16 * fr + 4 * fq) * 2) = (u32x2){pack2(z0, z1), pack2(z2, z3)};
    }
    WAIT_L(0);
  }
}

DEV void l2_prefetch(LAS char* scratch, const u16* x, const u16* wg, const u16* wu, int xcd, int hj) {
  const int lane = threadIdx.x & 63;
  const rsrc_t rx = mk_rsrc(x, S_ * D_ * 2), rg = mk_rsrc(wg, D_ * F_ * 2), ru = mk_rsrc(wu, D_ * F_ * 2);
  const unsigned long long t0 = __builtin_amdgcn_s_memtime();
  const unsigned vx = (unsigned)((xcd & 1) * 1024 + hj * 256 + lane) * 2048u;
  unsigned vw[4];
#pragma unroll
  for (int i = 0; i < 4; ++i) { int id = i * 64 + lane; id = id < 224 ? id : 223; vw[i] = (unsigned)(hj * 16 + id / 14) * (F_ * 2) + (xcd >> 1) * 1792 + (id % 14) * 128; }
  for (int kt = PF_FIRST; kt < 16; ++kt) {
    if (kt > PF_AHEAD) { const unsigned long long due = (unsigned long long)(kt - PF_AHEAD) * PF_PACE + PF_T0; while (__builtin_amdgcn_s_memtime() - t0 < due) __builtin_amdgcn_s_sleep(8); }
#pragma unroll
    for (int i = 0; i < 4; ++i) {
      __builtin_amdgcn_raw_ptr_buffer_load_lds(rg, (LAS void*)scratch, 4, vw[i], kt * 64 * F_ * 2, 0, 0);
      __builtin_amdgcn_raw_ptr_buffer_load_lds(ru, (LAS void*)scratch, 4, vw[i], kt * 64 * F_ * 2, 0, 0);
      __builtin_amdgcn_raw_ptr_buffer_load_lds(rx, (LAS void*)scratch, 4, vx + i * 64 * 2048u, kt * 128, 0, 0);
    }
  }
  WAIT_V(0);
}
__global__ __launch_bounds__(512, 2) void k1_kernel(const u16* __restrict__ q, const u16* __restrict__ kk, const u16* __restrict__ vv,
                                                    const u16* __restrict__ shk, const u16* __restrict__ ssmk, const u16* __restrict__ ssmD,
                                                    const u16* __restrict__ wg, const u16* __restrict__ wu, const u16* __restrict__ wd,
                                                    const u16* __restrict__ gw, char* __restrict__ ws) {
  extern __shared__ __attribute__((aligned(16))) char shm[];
  LAS char* lds = (LAS char*)shm;
  const int b = blockIdx.x, xcd = b & 7, j = b >> 3;
  if (j < 4) {
    conv_unit(lds, q, kk, vv, shk, ssmk, ssmD, wd, gw, ws, xcd * 4 + j);
  } else {
    const int jj = j - 4;
    const int pm = (xcd & 1) * 4 + (jj & 3), pn = (xcd >> 1) * 7 + (jj >> 2);
    gemm1_tile(lds, q, wg, wu, ws + WS_HF, pm, pn);
  }
}

#define PDV 2
constexpr int PD = PDV;
__global__ __launch_bounds__(256, 1) void k2_kernel(const u16* __restrict__ q, const char* __restrict__ ws, u16* __restrict__ out) {
  extern __shared__ __attribute__((aligned(16))) char shm[];
  LAS char* lds = (LAS char*)shm;
  const int tid = threadIdx.x, lane = tid & 63;
  const int wid = __builtin_amdgcn_readfirstlane(tid >> 6);
  const int fr = lane & 15, fq = lane >> 4;
  const int b = blockIdx.x, xcd = b & 7, j = b >> 3;
  const int rt = (xcd >> 1) * 4 + (j >> 3), ct = (xcd & 1) * 8 + (j & 7);
  const char* ha = ws + WS_HF + (size_t)(wid * 128 + rt * 8) * 1024;
  const char* wb = ws + WS_WDF + (size_t)(wid * 64 + ct * 4) * 1024;
  const unsigned lo16 = lane * 16;
  char* stamp_base = (char*)ws + (64u << 20) + 256 * 128;
  STAMP_AT(0);
  f32x4 acc[8][4];
#pragma unroll
  for (int i = 0; i < 8; ++i)
#pragma unroll
    for (int n = 0; n < 4; ++n) acc[i][n] = (f32x4){0.f, 0.f, 0.f, 0.f};
  s16x8 A[PD][8], B[PD][4];
  auto load_step = [&](int s, int slot) {
    const char* pa = ha + (size_t)s * (4 * 128 * 1024);
    const char* pb = wb + (size_t)s * (4 * 64 * 1024);
#pragma unroll
    for (int n = 0; n < 4; ++n) B[slot][n] = *(const s16x8*)(pb + n * 1024 + lo16);
#pragma unroll
    for (int i = 0; i < 8; ++i) A[slot][i] = *(const s16x8*)(pa + i * 1024 + lo16);
  };
#pragma unroll
  for (int s = 0; s < PD - 1; ++s) load_step(s, s);
#pragma unroll
  for (int s = 0; s < 28; ++s) {
    if (s + PD - 1 < 28) load_step(s + PD - 1, (s + PD - 1) % PD);
    SCHED();
#pragma unroll
    for (int i = 0; i < 8; ++i)
#pragma unroll
      for (int n = 0; n < 4; ++n) acc[i][n] = mfma16(B[s % PD][n], A[s % PD][i], acc[i][n]);
    SCHED();
  }
  STAMP_AT(4);
  {
    const char* src = ws + WS_ZT + (size_t)(ct * 64 + wid * 16 + (lane >> 4)) * 4096 + rt * 256 + (lane & 15) * 16;
#pragma unroll
    for (int i = 0; i < 4; ++i) glds16(src + (size_t)i * 4 * 4096, lds + 98304 + wid * 4096 + i * 1024);
  }
#pragma unroll
  for (int o = 0; o < 4; ++o) {
    if (o != wid) {
      const int slot = (wid < o) ? wid : wid - 1;
#pragma unroll
      for (int i2 = 0; i2 < 2; ++i2)
#pragma unroll
        for (int n = 0; n < 4; ++n)
          *(LAS f32x4*)(lds + ((o * 3 + slot) * 8 + i2 * 4 + n) * 1024 + lo16) = acc[o * 2 + i2][n];
    }
  }
  f32x4 fin[2][4];
#pragma unroll
  for (int o = 0; o < 4; ++o)
    if (o == wid) {
#pragma unroll
      for (int i2 = 0; i2 < 2; ++i2)
#pragma unroll
        for (int n = 0; n < 4; ++n) fin[i2][n] = acc[o * 2 + i2][n];
    }
  u32x4 qv[2][2]; float gv[2];
#pragma unroll
  for (int i2 = 0; i2 < 2; ++i2) {
    const int row = rt * 128 + (wid * 2 + i2) * 16 + fr;
    const u16* qp = q + (size_t)row * D_ + ct * 64 + fq * 16;
    qv[i2][0] = *(const u32x4*)qp; qv[i2][1] = *(const u32x4*)(qp + 8);
    gv[i2] = ((const float*)(ws + WS_G))[row];
  }
  WAIT_V(0); WAIT_L(0);
  STAMP_AT(5);
  __syncthreads();
  STAMP_AT(6);
#pragma unroll
  for (int s = 0; s < 3; ++s)
#pragma unroll
    for (int i2 = 0; i2 < 2; ++i2)
#pragma unroll
      for (int n = 0; n < 4; ++n) fin[i2][n] += *(LAS f32x4*)(lds + ((wid * 3 + s) * 8 + i2 * 4 + n) * 1024 + lo16);
  const int q4 = (lane & 15) >> 2, p = lane & 3;
#pragma unroll
  for (int i2 = 0; i2 < 2; ++i2) {
    const int row = rt * 128 + (wid * 2 + i2) * 16 + fr;
    const float g = gv[i2], og = rbf(1.0f - g);
    unsigned ow[8];
#pragma unroll
    for (int n = 0; n < 4; ++n) {
      LAS char* zp = lds + 98304 + (16 * fq + 4 * n + q4) * 256 + ((wid * 2 + i2) * 16 + 4 * p) * 2;
      const s16x4 zz = __builtin_amdgcn_ds_read_tr16_b64_v4i16((LAS s16x4*)zp);
      const u32x2 zu = __builtin_bit_cast(u32x2, zz);
      const unsigned q01 = qv[i2][n >> 1][(n & 1) * 2], q23 = qv[i2][n >> 1][(n & 1) * 2 + 1];
      float r4[4];
      const float zf[4] = {bflo(zu[0]), bfhi(zu[0]), bflo(zu[1]), bfhi(zu[1])};
      const float qf[4] = {bflo(q01), bfhi(q01), bflo(q23), bfhi(q23)};
#pragma unroll
      for (int r = 0; r < 4; ++r) {
        const float a = rbf(qf[r] * zf[r]);
        r4[r] = rbf(g * a) + rbf(og * rbf(fin[i2][n][r]));
      }
      ow[n * 2] = pack2(r4[0], r4[1]); ow[n * 2 + 1] = pack2(r4[2], r4[3]);
    }
    u16* op = out + (size_t)row * D_ + ct * 64 + fq * 16;
    *(u32x4*)op = (u32x4){ow[0], ow[1], ow[2], ow[3]};
    *(u32x4*)(op + 8) = (u32x4){ow[4], ow[5], ow[6], ow[7]};
  }
  WAIT_V(0);
  STAMP_AT(7);
}

extern "C" void kernel_launch(void* const* d_in, const int* in_sizes, int n_in, void* d_out, int out_size, void* d_ws, size_t ws_size,
                              hipStream_t stream) {
  const u16* q = (const u16*)d_in[0]; const u16* kk = (const u16*)d_in[1]; const u16* vv = (const u16*)d_in[2];
  const u16* shk = (const u16*)d_in[3]; const u16* ssmk = (const u16*)d_in[4]; const u16* ssmD = (const u16*)d_in[5];
  const u16* wg = (const u16*)d_in[7]; const u16* wu = (const u16*)d_in[8]; const u16* wd = (const u16*)d_in[9];
  const u16* gw = (const u16*)d_in[10];
  constexpr int LDS1 = 8 * CW_SIZE > 131072 ? 8 * CW_SIZE : 131072;
  constexpr int LDS2 = 98304 + 16384;
  hipFuncSetAttribute((const void*)k1_kernel, hipFuncAttributeMaxDynamicSharedMemorySize, LDS1);
  hipFuncSetAttribute((const void*)k2_kernel, hipFuncAttributeMaxDynamicSharedMemorySize, LDS2);
  k1_kernel<<<256, 512, LDS1, stream>>>(q, kk, vv, shk, ssmk, ssmD, wg, wu, wd, gw, (char*)d_ws);
  k2_kernel<<<256, 256, LDS2, stream>>>(q, (const char*)d_ws, (u16*)d_out);
}
```

```cpp
#include <hip/hip_runtime.h>
#include <hip/hip_bf16.h>

typedef unsigned short u16;
typedef unsigned long long u64;
typedef short s16x4 __attribute__((ext_vector_type(4)));
typedef short s16x8 __attribute__((ext_vector_type(8)));
typedef __bf16 bfx2 __attribute__((ext_vector_type(2)));
typedef float f32x4 __attribute__((ext_vector_type(4)));
typedef unsigned u32x4 __attribute__((ext_vector_type(4)));
typedef unsigned u32x2 __attribute__((ext_vector_type(2)));
typedef __amdgpu_buffer_rsrc_t rsrc_t;
#define LAS __attribute__((address_space(3)))
#define GAS __attribute__((address_space(1)))
#define DEV __device__ __forceinline__
extern "C" __device__ u64 hw_dispatch_id() __asm("llvm.amdgcn.dispatch.id");

#define LATE_PRIO 0
constexpr int S_ = 2048, D_ = 1024, F_ = 3584;
constexpr unsigned WS_HF = 0;
constexpr unsigned WS_KVT = 16u << 20;
constexpr unsigned WS_ZT = 20u << 20;
constexpr unsigned WS_G = 24u << 20;
constexpr unsigned WS_FLAG = 25u << 20;
constexpr unsigned WS_BYTES = 26u << 20;
constexpr int FL_H = 0, FL_G = 1024, FL_Z = 1536, FL_KV = 2048, FL_BYTES = 2560;
constexpr unsigned FL_STRIDE = 4096;
constexpr int SC1 = 16;
constexpr int L_PFS = 163840 - 512, L_CTRL = 163840 - 64;

DEV float bflo(unsigned w) { return __uint_as_float(w << 16); }
DEV float bfhi(unsigned w) { return __uint_as_float(w & 0xffff0000u); }
DEV unsigned pack2(float lo, float hi) { bfx2 p; p[0] = (__bf16)lo; p[1] = (__bf16)hi; return __builtin_bit_cast(unsigned, p); }
DEV float rbf(float f) { return (float)(__bf16)f; }
DEV void mfma16a(f32x4& c, s16x8 a, s16x8 b) {
  asm volatile("v_mfma_f32_16x16x32_bf16 %0, %1, %2, %0" : "+v"(c) : "v"(a), "v"(b));
}
DEV void mfma16z(f32x4& c, s16x8 a, s16x8 b) {
  asm volatile("v_mfma_f32_16x16x32_bf16 %0, %1, %2, 0" : "=&v"(c) : "v"(a), "v"(b));
}
DEV s16x8 tr_read8(LAS char* p0, LAS char* p1) {
  s16x4 lo = __builtin_amdgcn_ds_read_tr16_b64_v4i16((LAS s16x4*)p0);
  s16x4 hi = __builtin_amdgcn_ds_read_tr16_b64_v4i16((LAS s16x4*)p1);
  return __builtin_shufflevector(lo, hi, 0, 1, 2, 3, 4, 5, 6, 7);
}
template <int OFF, int D2>
DEV s16x8 tr_read8_asm(unsigned addr) {
  s16x4 lo, hi;
  asm volatile("ds_read_b64_tr_b16 %0, %2 offset:%3\n\tds_read_b64_tr_b16 %1, %2 offset:%4" : "=&v"(lo), "=&v"(hi) : "v"(addr), "i"(OFF), "i"(OFF + D2));
  return __builtin_shufflevector(lo, hi, 0, 1, 2, 3, 4, 5, 6, 7);
}
template <int OFF>
DEV s16x8 ds_read16_asm(unsigned addr) {
  s16x8 r;
  asm volatile("ds_read_b128 %0, %1 offset:%2" : "=v"(r) : "v"(addr), "i"(OFF));
  return r;
}
template <int V> struct IC { static constexpr int value = V; };
DEV rsrc_t mk_rsrc(const void* p, unsigned bytes) { return __builtin_amdgcn_make_buffer_rsrc((void*)p, 0, bytes, 0x00020000); }
DEV void blds16(rsrc_t r, LAS char* l, unsigned voff, unsigned soff) {
  __builtin_amdgcn_raw_ptr_buffer_load_lds(r, (LAS void*)l, 16, voff, soff, 0, 0);
}
DEV void blds4(rsrc_t r, LAS char* l, unsigned voff, unsigned soff) {
  __builtin_amdgcn_raw_ptr_buffer_load_lds(r, (LAS void*)l, 4, voff, soff, 0, 0);
}
DEV u32x4 ld16(rsrc_t r, unsigned voff, unsigned soff) { return __builtin_amdgcn_raw_buffer_load_b128(r, voff, soff, SC1); }
DEV void st16(rsrc_t r, unsigned voff, u32x4 v) { __builtin_amdgcn_raw_buffer_store_b128(v, r, voff, 0, SC1); }
#define STAMP_AT(i)
#define WAIT_V(n) asm volatile("s_waitcnt vmcnt(" #n ")" ::: "memory")
#define WAIT_L(n) asm volatile("s_waitcnt lgkmcnt(" #n ")" ::: "memory")
#define BAR() __builtin_amdgcn_s_barrier()
#define SCHED() __builtin_amdgcn_sched_barrier(0)
#define MFMA_DRAIN() do { asm volatile("s_nop 7\n\ts_nop 7" ::: "memory"); SCHED(); } while (0)

DEV void flag_set(char* ws, unsigned off, u64 tag) { __hip_atomic_store((GAS u64*)(ws + off), tag, __ATOMIC_RELAXED, __HIP_MEMORY_SCOPE_AGENT); }
DEV u64 flag_peek(char* ws, unsigned off, int n) {
  const int lane = threadIdx.x & 63;
  return __hip_atomic_load((GAS u64*)(ws + off + 8 * (lane < n ? lane : 0)), __ATOMIC_RELAXED, __HIP_MEMORY_SCOPE_AGENT);
}
template <int SLP>
DEV void flag_wait(u64 first, char* ws, unsigned off, int n, u64 tag) {
  u64 v = first;
  for (unsigned spins = 0; spins < (1u << 16); ++spins) {
    if (__all(v == tag)) break;
    __builtin_amdgcn_s_sleep(SLP);
    v = flag_peek(ws, off, n);
  }
  __builtin_amdgcn_fence(__ATOMIC_ACQUIRE, "wavefront");
}
template <int SLP = 1>
DEV void lds_wait4(LAS char* p) {
  for (unsigned spins = 0; spins < (1u << 22); ++spins) {
    const u32x4 dn = *(volatile LAS u32x4*)p;
    if (__builtin_amdgcn_readfirstlane(dn[0] & dn[1] & dn[2] & dn[3]) != 0u) break;
    __builtin_amdgcn_s_sleep(SLP);
  }
  asm volatile("" ::: "memory");
}
DEV int tile_of_block(int b) { const int xcd = b & 7, j = b >> 3; return ((xcd >> 1) * 4 + (j >> 3)) * 16 + (xcd & 1) * 8 + (j & 7); }

struct Args {
  const u16 *q, *kk, *vv, *shk, *ssmk, *ssmD, *wg, *wu, *wd, *gw;
  char* ws; u16* out;
};

DEV void gemm1_main(f32x4 (&acc)[4][8], u64& kvpeek, unsigned kvoff, LAS char* lds, const Args& a, int pm, int pn, int im, int in_) {
  char* ws = a.ws;
  const int tid = threadIdx.x, lane = tid & 63;
  const int wid = __builtin_amdgcn_readfirstlane(tid >> 6);
  const int wr = wid >> 1, wc = wid & 1;
  const int fr = lane & 15, fq = lane >> 4;
  unsigned va[2], vb[2];
#pragma unroll
  for (int e = 0; e < 2; ++e) {
    const int rl = lane >> 3, p = lane & 7;
    const int sw = (e << 2) | (lane >> 4);
    va[e] = (unsigned)(rl * 2048 + ((p ^ sw) * 16));
    const int kl = lane >> 4, pb = lane & 15;
    const int swb = (kl | (e << 2)) << 1;
    vb[e] = (unsigned)(kl * (F_ * 2) + (pb ^ swb) * 16);
  }
  const rsrc_t rx = mk_rsrc(a.q, S_ * D_ * 2);
  const rsrc_t rw = mk_rsrc((wid < 4) ? a.wg : a.wu, D_ * F_ * 2);
  const unsigned xa_base = (unsigned)(pm * 256 + wid * 32) * 2048u;
  const unsigned wb_base = (unsigned)((wid & 3) * 16) * (F_ * 2) + pn * 256;
  LAS char* ldsA = lds + wid * 4096;
  LAS char* ldsB = lds + 65536 + wid * 4096;
  auto dma = [&](int kt, int st, int d) {
    const int i = d >> 1;
    if (d & 1) blds16(rw, ldsB + st * 32768 + i * 1024, vb[i >> 1], wb_base + (kt * 64 + i * 4) * (F_ * 2));
    else blds16(rx, ldsA + st * 32768 + i * 1024, va[i & 1], xa_base + i * (8 * 2048) + kt * 128);
  };
#pragma unroll
  for (int d = 0; d < 8; ++d) dma(0, 0, d);
#pragma unroll
  for (int d = 0; d < 8; ++d) dma(1, 1, d);
  SCHED();
  rsrc_t rp; unsigned vp, pstep;
  if (wid < 4) {
    rp = mk_rsrc((im >> 1) ? a.wu : a.wg, D_ * F_ * 2);
    vp = (unsigned)(wid * 16 + (lane & 15)) * (F_ * 2) + pn * 256 + (im & 1) * 128; pstep = 64 * F_ * 2;
  } else {
    rp = rx;
    int r = in_ * 37 + (wid - 4) * 10 + ((lane & 15) < 10 ? (lane & 15) : 9); r = r > 255 ? 255 : r;
    vp = (unsigned)(pm * 256 + r) * 2048u; pstep = 128;
  }
  vp += (unsigned)(lane >> 4) * 32u;
  auto pf = [&](int kt) { blds4(rp, lds + L_PFS, vp, kt * pstep); };
  const unsigned lds0 = (unsigned)(size_t)lds;
  unsigned bx[2];
#pragma unroll
  for (int ks = 0; ks < 2; ++ks) bx[ks] = lds0 + (wr * 64 + fr) * 128 + (((ks * 4 + fq) ^ (fr >> 1)) * 16);
  unsigned bw[4];
  {
    const int q = (lane & 15) >> 2, p = lane & 3;
    const int sw = (q | ((fq & 1) << 2)) << 1;
#pragma unroll
    for (int j = 0; j < 4; ++j) bw[j] = lds0 + 65536 + (8 * fq + q) * 256 + (((wc * 8 + j * 2 + (p >> 1)) ^ sw) * 16) + (p & 1) * 8;
  }
  auto ldx = [&](auto ST, auto KS, const int i) -> s16x8 {
    constexpr int o = decltype(ST)::value * 32768; const unsigned p = bx[decltype(KS)::value];
    return i == 0 ? ds_read16_asm<o>(p) : i == 1 ? ds_read16_asm<o + 2048>(p) : i == 2 ? ds_read16_asm<o + 4096>(p) : ds_read16_asm<o + 6144>(p);
  };
  auto ldw = [&](auto ST, auto KS, const int n) -> s16x8 {
    constexpr int o = decltype(ST)::value * 32768 + decltype(KS)::value * 8192;
    return n < 4 ? tr_read8_asm<o, 1024>(bw[n & 3]) : tr_read8_asm<o + 16384, 1024>(bw[n & 3]);
  };
  s16x8 xa[4], xb[4], w[8];
  WAIT_V(8); BAR(); SCHED();
  STAMP_AT(1);
#pragma unroll
  for (int i = 0; i < 4; ++i) xa[i] = ldx(IC<0>{}, IC<0>{}, i);
#pragma unroll
  for (int n = 0; n < 8; ++n) w[n] = ldw(IC<0>{}, IC<0>{}, n);
  pf(2); pf(3); pf(4);
  WAIT_L(0);
  SCHED();

  auto tile_body = [&](auto ST, const int kt, const bool pre1, const bool pre2, const int hook) {
    constexpr int st = decltype(ST)::value;
#pragma unroll
    for (int n = 0; n < 8; ++n) {
      if (pre2) { if (n == 2) __builtin_amdgcn_s_setprio(1); if (n == 6) __builtin_amdgcn_s_setprio(0); }
      if (n == 6) WAIT_L(15);
#pragma unroll
      for (int i = 0; i < 4; ++i) {
        if (n == 0) { if (i == 0) WAIT_L(13); if (i == 1) WAIT_L(10); if (i == 2) WAIT_L(7); if (i == 3) WAIT_L(4); }
        if (hook == 1) mfma16z(acc[i][n], w[n], xa[i]); else mfma16a(acc[i][n], w[n], xa[i]);
      }
      w[n] = ldw(ST, IC<1>{}, n);
      if (n < 4) xb[n] = ldx(ST, IC<1>{}, n);
      if (n == 3 && hook == 0) pf(kt + 4);
      if (n == 3 && hook == 5) kvpeek = flag_peek(ws, kvoff, 32);
      SCHED();
    }
#pragma unroll
    for (int n = 0; n < 2; ++n) {
#pragma unroll
      for (int i = 0; i < 4; ++i) {
        if (n == 0) { if (i == 0) WAIT_L(15); if (i == 1) WAIT_L(14); if (i == 2) WAIT_L(11); if (i == 3) WAIT_L(8); }
        mfma16a(acc[i][n], w[n], xb[i]);
      }
    }
    SCHED();
    WAIT_L(0);
    if (pre1) {
      if (hook == 1) { WAIT_V(3); } else if (hook == 7) { WAIT_V(0); } else { WAIT_V(1); }
      BAR(); SCHED();
      if (pre2) __builtin_amdgcn_s_setprio(3);
      else if (wid < 4) __builtin_amdgcn_s_setprio(2);
      else __builtin_amdgcn_s_setprio(1);
      w[0] = ldw(IC<(st ^ 1)>{}, IC<0>{}, 0); w[1] = ldw(IC<(st ^ 1)>{}, IC<0>{}, 1);
      if (pre2) { dma(kt + 2, st, 0); dma(kt + 2, st, 1); }
      SCHED();
    }
#pragma unroll
    for (int n = 2; n < 8; ++n) {
      if (pre2 && n == 6) __builtin_amdgcn_s_setprio(2);
#pragma unroll
      for (int i = 0; i < 4; ++i) mfma16a(acc[i][n], w[n], xb[i]);
      if (pre1) {
        w[n] = ldw(IC<(st ^ 1)>{}, IC<0>{}, n);
        if (n - 2 < 4) xa[n - 2] = ldx(IC<(st ^ 1)>{}, IC<0>{}, n - 2);
        if (pre2) dma(kt + 2, st, n);
      }
      SCHED();
    }
  };
  tile_body(IC<0>{}, 0, true, true, 1);
  tile_body(IC<1>{}, 1, true, true, 0);
  for (int kt = 2; kt < 12; kt += 2) {
    tile_body(IC<0>{}, kt, true, true, 0);
    tile_body(IC<1>{}, kt + 1, true, true, 0);
  }
  tile_body(IC<0>{}, 12, true, true, 5);
  tile_body(IC<1>{}, 13, true, true, 7);
  tile_body(IC<0>{}, 14, true, false, 7);
  tile_body(IC<1>{}, 15, false, false, 7);
  *(volatile LAS unsigned*)(lds + L_CTRL + wid * 4) = 1u;
  MFMA_DRAIN();
  STAMP_AT(2);
}

DEV void light_unit(LAS char* lds, const Args& a, rsrc_t rws, u64 tag, int cu) {
  char* ws = a.ws;
  const int tid = threadIdx.x, lane = tid & 63;
  const int wid = __builtin_amdgcn_readfirstlane(tid >> 6);
  const int cb = cu & 7, tblk = (cu >> 3) * 8 + wid;
  const int kch = cb * 128 + lane * 2;
  LAS char* wl = lds + wid * (128 * 144);
  const u32x4 shw = *(const u32x4*)(a.shk + kch * 4);
#pragma unroll
  for (int hf = 0; hf < 2; ++hf) {
    const int t0 = tblk * 64 + hf * 32;
    unsigned kr[35], vr[32];
#pragma unroll
    for (int jx = 0; jx < 35; ++jx) { const int t = t0 - 3 + jx; kr[jx] = *(const unsigned*)(a.kk + (size_t)(t < 0 ? 0 : t) * D_ + kch); }
#pragma unroll
    for (int jx = 0; jx < 32; ++jx) vr[jx] = *(const unsigned*)(a.vv + (size_t)(t0 + jx) * D_ + kch);
    SCHED();
#pragma unroll
    for (int c = 0; c < 2; ++c) {
      const unsigned s01 = shw[c * 2], s23 = shw[c * 2 + 1];
      const float w0 = bflo(s01), w1 = bfhi(s01), w2 = bflo(s23), w3 = bfhi(s23);
#pragma unroll
      for (int sb = 0; sb < 4; ++sb) {
        float kf[11];
#pragma unroll
        for (int jx = 0; jx < 11; ++jx) { kf[jx] = c ? bfhi(kr[sb * 8 + jx]) : bflo(kr[sb * 8 + jx]); if (sb == 0 && jx < 3 && t0 == 0) kf[jx] = 0.f; }
        float o[8];
#pragma unroll
        for (int jx = 0; jx < 8; ++jx) o[jx] = (kf[jx] * w3 + kf[jx + 1] * w2 + kf[jx + 2] * w1 + kf[jx + 3] * w0) * (c ? bfhi(vr[sb * 8 + jx]) : bflo(vr[sb * 8 + jx]));
        *(LAS u32x4*)(wl + (lane * 2 + c) * 144 + (hf * 4 + sb) * 16) = (u32x4){pack2(o[7], o[6]), pack2(o[5], o[4]), pack2(o[3], o[2]), pack2(o[1], o[0])};
      }
    }
  }
  WAIT_L(0);
#pragma unroll
  for (int e = 0; e < 16; ++e) {
    const int c = e * 8 + (lane >> 3);
    const u32x4 v = *(LAS u32x4*)(wl + c * 144 + (lane & 7) * 16);
    st16(rws, WS_KVT + (unsigned)(cb * 128 + c) * 4096u + tblk * 128 + (lane & 7) * 16, v);
  }
  WAIT_V(0);
  flag_set(ws, WS_FLAG + (unsigned)tile_of_block(cb * 32 + (lane & 31)) * FL_STRIDE + FL_KV + tblk * 8, tag);
  STAMP_AT(1);
  const rsrc_t rd = mk_rsrc(a.wd, F_ * D_ * 2), rk = mk_rsrc(a.ssmk, D_ * S_ * 2);
#pragma unroll
  for (int i = 0; i < 2; ++i) blds4(rk, lds + L_PFS, tid * 128, cu * 131072 + i * 65536);
#pragma unroll
  for (int i = 0; i < 4; ++i) blds4(rd, lds + L_PFS, tid * 128, (i * 32 + cu) * 57344);
  WAIT_V(0);
  STAMP_AT(2);
  __syncthreads();
}

DEV u32x2 geglu4(f32x4 gt, f32x4 up) {
  const float C1 = -2.302208198f, C2 = -2.302208198f * 0.044715f;
  const f32x4 wv = gt * (gt * gt * C2 + C1);
  f32x4 ev;
#pragma unroll
  for (int r = 0; r < 4; ++r) ev[r] = __builtin_amdgcn_exp2f(wv[r]);
  const f32x4 dv = ev + 1.0f;
  f32x4 rv;
#pragma unroll
  for (int r = 0; r < 4; ++r) rv[r] = __builtin_amdgcn_rcpf(dv[r]);
  const f32x4 hv = gt * up * rv;
  return (u32x2){pack2(hv[0], hv[1]), pack2(hv[2], hv[3])};
}
DEV void gemm1_epi(const f32x4 (&acc)[4][8], rsrc_t rws, int pm, int pn) {
  const int lane = threadIdx.x & 63;
  const int wid = __builtin_amdgcn_readfirstlane(threadIdx.x >> 6);
  const int wr = wid >> 1, wc = wid & 1;
  const unsigned off = WS_HF + (unsigned)((pn * 4 + wc * 2) * 128 + pm * 16 + wr * 4) * 1024u + lane * 16;
#pragma unroll
  for (int i = 0; i < 4; ++i)
#pragma unroll
    for (int k2 = 0; k2 < 2; ++k2) {
      const u32x2 h0 = geglu4(acc[i][k2 * 2], acc[i][4 + k2 * 2]), h1 = geglu4(acc[i][k2 * 2 + 1], acc[i][4 + k2 * 2 + 1]);
      st16(rws, off + k2 * 131072 + i * 1024, (u32x4){h0[0], h0[1], h1[0], h1[1]});
    }
}

constexpr int CV_CS = 4160, CV_U = 4 * CV_CS  , CV_SIZE = 23552;
struct ConvIn { u32x4 own[4]; u32x2 nxt[4], h0; u32x4 u[4]; };

DEV void conv_write_seg(LAS char* wl, int seg, u32x4 a, u32x2 b) {
  const unsigned a01 = __builtin_amdgcn_alignbit(a[1], a[0], 16), a12 = __builtin_amdgcn_alignbit(a[2], a[1], 16),
                 a23 = __builtin_amdgcn_alignbit(a[3], a[2], 16), a34 = __builtin_amdgcn_alignbit(b[0], a[3], 16),
                 a45 = __builtin_amdgcn_alignbit(b[1], b[0], 16);
  LAS char* dst = wl + seg * 16;
  *(LAS u32x4*)(dst) = a;
  *(LAS u32x4*)(dst + CV_CS) = (u32x4){a01, a12, a23, a34};
  *(LAS u32x4*)(dst + 2 * CV_CS) = (u32x4){a[1], a[2], a[3], b[0]};
  *(LAS u32x4*)(dst + 3 * CV_CS) = (u32x4){a12, a23, a34, a45};
}
DEV s16x8 conv_ldp(LAS char* p) {
  volatile LAS s16x4* pp = (volatile LAS s16x4*)p;
  const s16x4 lo = pp[0], hi = pp[1];
  return __builtin_shufflevector(lo, hi, 0, 1, 2, 3, 4, 5, 6, 7);
}

DEV void conv_wave(LAS char* wl, const ConvIn& in, float dv, rsrc_t rws, int ch) {
  const int lane = threadIdx.x & 63;
  const int fr = lane & 15, fq = lane >> 4;
  const u32x4 zero4 = (u32x4){0u, 0u, 0u, 0u};
#pragma unroll
  for (int p = 0; p < 4; ++p) conv_write_seg(wl, 64 * p + lane + 2, in.own[p], in.nxt[p]);
  conv_write_seg(wl, lane & 1, zero4, (lane & 1) ? in.h0 : (u32x2){0u, 0u});
  *(LAS u32x4*)(wl + CV_U + (lane & 31) * 16) = zero4;
#pragma unroll
  for (int i = 0; i < 4; ++i) *(LAS u32x4*)(wl + CV_U + 512 + i * 1024 + lane * 16) = in.u[i];
  const int o_ = 1 + fr + 8 * fq;
  LAS char* pb = wl + (o_ & 3) * CV_CS + 2 * (o_ & ~3);
  LAS char* qb = wl + CV_U + 16 * (33 + 2 * fr - fq);
  f32x4 acc[8];
#pragma unroll
  for (int g = 0; g < 8; ++g) acc[g] = (f32x4){0.f, 0.f, 0.f, 0.f};
  s16x8 P0[8], Q0[8], P1[8], Q1[8];
  WAIT_L(0); SCHED();
#pragma unroll
  for (int a = 0; a < 8; ++a) { P0[a] = conv_ldp(pb + a * 512); Q0[a] = *(LAS s16x8*)(qb + a * 512); }
  SCHED();
  auto cls = [&](s16x8 (&P)[8], s16x8 (&Q)[8], s16x8 (&Pn)[8], s16x8 (&Qn)[8], LAS char* pn, LAS char* qn) {
#pragma unroll
    for (int b = 0; b < 8; ++b) {
      Pn[b] = conv_ldp(pn + b * 512); Qn[b] = *(LAS s16x8*)(qn + b * 512);
#pragma unroll
      for (int a = 0; a + b < 8; ++a) mfma16a(acc[a + b], P[a], Q[b]);
      SCHED();
    }
  };
  for (int r = 0; r < 8; r += 2) {
    cls(P0, Q0, P1, Q1, pb + (r + 1) * 64, qb - (r + 1) * 64);
    const int rn = (r + 2) & 7;
    cls(P1, Q1, P0, Q0, pb + rn * 64, qb - rn * 64);
  }
  MFMA_DRAIN();
#pragma unroll
  for (int e = 0; e < 4; ++e) {
    unsigned zx[2][2];
#pragma unroll
    for (int h = 0; h < 2; ++h) {
      const int g = 2 * e + h;
      const int al = 32 * g + 2 * fr + (fq >> 1);
      const u32x2 kvp = *(LAS u32x2*)(wl + CV_U + 512 + al * 16 + ((fq & 1) ? 0 : 8));
      const float k3 = bflo(kvp[0]), k2 = bfhi(kvp[0]), k1 = bflo(kvp[1]), k0 = bfhi(kvp[1]);
      const float z0 = rbf(acc[g][0]) + rbf(k0 * dv), z1 = rbf(acc[g][1]) + rbf(k1 * dv), z2 = rbf(acc[g][2]) + rbf(k2 * dv), z3 = rbf(acc[g][3]) + rbf(k3 * dv);
      zx[h][0] = pack2(z0, z1); zx[h][1] = pack2(z2, z3);
    }
    const auto r0 = __builtin_amdgcn_permlane16_swap(zx[0][0], zx[1][0], false, false);
    const auto r1 = __builtin_amdgcn_permlane16_swap(zx[0][1], zx[1][1], false, false);
    st16(rws, WS_ZT + (unsigned)ch * 4096u + (unsigned)(256 * (2 * e + (fq & 1)) + 16 * fr + 8 * (fq >> 1)) * 2u, (u32x4){r0[0], r1[0], r0[1], r1[1]});
  }
}

constexpr int L_STG = 8192, L_ZS = L_STG + 9216, L_RING = 4 * CV_SIZE;
static_assert(L_ZS + 5120 <= CV_SIZE && CV_U + 4608 <= CV_SIZE && L_RING + 65536 <= L_PFS, "lds map");

__global__ __launch_bounds__(512, 2) void k1_kernel(const u16* __restrict__ q, const u16* __restrict__ kk, const u16* __restrict__ vv,
                                                    const u16* __restrict__ shk, const u16* __restrict__ ssmk, const u16* __restrict__ ssmD,
                                                    const u16* __restrict__ wg, const u16* __restrict__ wu, const u16* __restrict__ wd,
                                                    const u16* __restrict__ gw, char* ws, u16* __restrict__ out) {
  extern __shared__ __attribute__((aligned(16))) char shm[];
  LAS char* lds = (LAS char*)shm;
  const int tid = threadIdx.x, lane = tid & 63;
  const int wid = __builtin_amdgcn_readfirstlane(tid >> 6);
  const int fr = lane & 15, fq = lane >> 4;
  const int b = blockIdx.x, xcd = b & 7, j = b >> 3;
  const u64 tag = ((hw_dispatch_id() * 0x9E3779B97F4A7C15ull) ^ 0xD1B54A32D192ED03ull) | 1ull;
  const rsrc_t rws = mk_rsrc(ws, WS_BYTES);
  const int rt = (xcd >> 1) * 4 + (j >> 3), ct = (xcd & 1) * 8 + (j & 7);
  const unsigned myfl = WS_FLAG + (unsigned)(rt * 16 + ct) * FL_STRIDE;
  const int ch = b * 4 + (wid & 3);
  const bool light = j < 4;
  const int jj = light ? 0 : j - 4;
  const int pm = (xcd & 1) * 4 + (jj & 3), pn = (xcd >> 1) * 7 + (jj >> 2);
  const Args a{q, kk, vv, shk, ssmk, ssmD, wg, wu, wd, gw, ws, out};
  STAMP_AT(0);

  if (tid < 16) *(volatile LAS unsigned*)(lds + L_CTRL + tid * 4) = (light && tid < 8) ? 1u : 0u;
  ConvIn in;
  const u16* hrow = ssmk + (size_t)ch * S_;
  unsigned dvraw = 0;
  auto conv_issue_h = [&]() {
#pragma unroll
    for (int p = 0; p < 4; ++p) {
      in.own[p] = *(const u32x4*)(hrow + 8 * (64 * p + lane));
      const int nx = 64 * p + lane + 1;
      in.nxt[p] = *(const u32x2*)(hrow + 8 * (nx < 256 ? nx : 255));
    }
    in.h0 = *(const u32x2*)hrow;
    dvraw = ssmD[ch];
  };
  auto conv_issue_u = [&]() {
#pragma unroll
    for (int i = 0; i < 4; ++i) in.u[i] = ld16(rws, lane * 16 + i * 1024, WS_KVT + (unsigned)ch * 4096u);
  };
  u64 kv0 = 0;
  if (light) {
    light_unit(lds, a, rws, tag, xcd * 4 + j);
    conv_issue_h();
    flag_wait<4>(kv0, ws, myfl + FL_KV, 32, tag);
    conv_issue_u();
  } else {
    f32x4 acc1[4][8];
    gemm1_main(acc1, kv0, myfl + FL_KV, lds, a, pm, pn, jj & 3, jj >> 2);
    flag_wait<4>(kv0, ws, myfl + FL_KV, 32, tag);
    conv_issue_h();
    conv_issue_u();
    SCHED();
    gemm1_epi(acc1, rws, pm, pn);
    if (wid < 4) __builtin_amdgcn_s_setprio(0);
    WAIT_V(0);
    flag_set(ws, WS_FLAG + (unsigned)((pm * 2 + (wid >> 2)) * 16 + (lane & 15)) * FL_STRIDE + FL_H + (((wid & 1) * 28 + pn) * 2 + ((wid >> 1) & 1)) * 8, tag);
  }
  STAMP_AT(3);
  lds_wait4(lds + L_CTRL); lds_wait4(lds + L_CTRL + 16);

  if (wid >= 4) {
    const int w4 = wid - 4;
    const unsigned lo16 = lane * 16;
    const unsigned ha = WS_HF + (unsigned)(w4 * 128 + rt * 8) * 1024u;
    const rsrc_t rd = mk_rsrc(wd, F_ * D_ * 2);
    unsigned vbs[2];
#pragma unroll
    for (int gp = 0; gp < 2; ++gp)
      vbs[gp] = (unsigned)((lane >> 3) & 3) * 2048u + (((lane & 7) ^ ((((lane >> 4) & 1) | (gp << 1)) << 1)) * 16) + (lane >> 5) * (16 * 2048u);
    LAS char* ring = lds + L_RING + w4 * 16384;
    unsigned bb[4];
    {
      const int q4 = (lane & 15) >> 2, p = lane & 3;
      const int f2 = (q4 >> 1) | ((fq & 1) << 1);
#pragma unroll
      for (int n = 0; n < 4; ++n) bb[n] = (unsigned)(size_t)ring + (8 * fq + q4) * 128 + ((((n ^ f2) << 1) + (p >> 1)) * 16) + (p & 1) * 8;
    }
    s16x8 A[2][8], B[2][4];
    auto ldA = [&](int s, int i) { return __builtin_bit_cast(s16x8, ld16(rws, lo16 + (i & 3) * 1024, ha + (unsigned)s * (4 * 128 * 1024) + (i >> 2) * 4096)); };
    auto dmaB = [&](int s, int slot) {
#pragma unroll
      for (int g = 0; g < 4; ++g) blds16(rd, ring + slot * 4096 + g * 1024, vbs[g & 1], (unsigned)((4 * s + w4) * 32 + 4 * g) * 2048u + ct * 128);
    };
    auto ldB = [&](const int slot, int n) {
      return slot == 0 ? tr_read8_asm<0, 512>(bb[n]) : slot == 1 ? tr_read8_asm<4096, 512>(bb[n]) : slot == 2 ? tr_read8_asm<8192, 512>(bb[n]) : tr_read8_asm<12288, 512>(bb[n]);
    };
    dmaB(0, 0); dmaB(1, 1); dmaB(2, 2);
    f32x4 acc[8][4];
#pragma unroll
    for (int i = 0; i < 8; ++i)
#pragma unroll
      for (int n = 0; n < 4; ++n) acc[i][n] = (f32x4){0.f, 0.f, 0.f, 0.f};
    flag_wait<4>(flag_peek(ws, myfl + FL_H + (w4 >> 1) * 448, 56), ws, myfl + FL_H + (w4 >> 1) * 448, 56, tag);
    WAIT_V(0);
    STAMP_AT(4);
    __builtin_amdgcn_s_setprio(3);
#pragma unroll
    for (int s = 0; s < 2; ++s)
#pragma unroll
      for (int i = 0; i < 8; ++i) A[s][i] = ldA(s, i);
#pragma unroll
    for (int n = 0; n < 4; ++n) B[0][n] = ldB(0, n);
    SCHED();
    auto step = [&](const int sq, const int s, const bool pre3, const bool pre2, const bool pre1) {
      const int sl = sq & 1;
      WAIT_L(0);
      if (pre3) dmaB(s + 3, (sq + 3) & 3);
      if (pre1) {
        if (pre3) { WAIT_V(24); } else if (pre2) { WAIT_V(20); } else { WAIT_V(8); }
#pragma unroll
        for (int n = 0; n < 4; ++n) B[sl ^ 1][n] = ldB((sq + 1) & 3, n);
      }
      SCHED();
#pragma unroll
      for (int i = 0; i < 8; ++i) {
#pragma unroll
        for (int n = 0; n < 4; ++n) mfma16a(acc[i][n], B[sl][n], A[sl][i]);
        if (pre2) A[sl][i] = ldA(s + 2, i);
        SCHED();
      }
    };
    for (int s = 0; s < 12; s += 4) { step(0, s, true, true, true); step(1, s + 1, true, true, true); step(2, s + 2, true, true, true); step(3, s + 3, true, true, true); }
    STAMP_AT(5);
    *(volatile LAS unsigned*)(lds + L_CTRL + 48 + w4 * 4) = 1u;
    for (int s = 12; s < 24; s += 4) { step(0, s, true, true, true); step(1, s + 1, true, true, true); step(2, s + 2, true, true, true); step(3, s + 3, true, true, true); }
    step(0, 24, true, true, true); step(1, 25, false, true, true); step(2, 26, false, false, true); step(3, 27, false, false, false);
    MFMA_DRAIN();
    STAMP_AT(6);
    lds_wait4(lds + L_CTRL + 32);
#pragma unroll
    for (int o = 0; o < 4; ++o) {
      if (o != w4) {
        const int d = (o < w4) ? o : o - 1;
        LAS char* dst = (d < 2) ? ring + d * 8192 : lds + w4 * CV_SIZE;
#pragma unroll
        for (int i2 = 0; i2 < 2; ++i2)
#pragma unroll
          for (int n = 0; n < 4; ++n) *(LAS f32x4*)(dst + (i2 * 4 + n) * 1024 + lo16) = acc[o * 2 + i2][n];
      }
    }
    f32x4 fin[2][4];
#pragma unroll
    for (int o = 0; o < 4; ++o)
      if (o == w4) {
#pragma unroll
        for (int i2 = 0; i2 < 2; ++i2)
#pragma unroll
          for (int n = 0; n < 4; ++n) fin[i2][n] = acc[o * 2 + i2][n];
      }
    WAIT_L(0);
    BAR();
    asm volatile("" ::: "memory");
#pragma unroll
    for (int k = 1; k < 4; ++k) {
      const int sw = (w4 + k) & 3;
      const int d = (w4 < sw) ? w4 : w4 - 1;
      LAS char* src = (d < 2) ? lds + L_RING + sw * 16384 + d * 8192 : lds + sw * CV_SIZE;
#pragma unroll
      for (int i2 = 0; i2 < 2; ++i2)
#pragma unroll
        for (int n = 0; n < 4; ++n) fin[i2][n] += *(LAS f32x4*)(src + (i2 * 4 + n) * 1024 + lo16);
    }
    LAS char* sg = lds + w4 * CV_SIZE + L_STG;
#pragma unroll
    for (int i2 = 0; i2 < 2; ++i2) {
      const int row = rt * 128 + (w4 * 2 + i2) * 16 + fr;
      const float og = *(LAS float*)(sg + 8192 + i2 * 256 + lane * 4);
      unsigned ow[4][2];
#pragma unroll
      for (int n = 0; n < 4; ++n) {
        const f32x4 ga = *(LAS f32x4*)(sg + (i2 * 4 + n) * 1024 + lo16);
        float r4[4];
#pragma unroll
        for (int r = 0; r < 4; ++r) r4[r] = ga[r] + rbf(og * rbf(fin[i2][n][r]));
        ow[n][0] = pack2(r4[0], r4[1]); ow[n][1] = pack2(r4[2], r4[3]);
      }
#pragma unroll
      for (int n2 = 0; n2 < 2; ++n2) {
        const auto r0 = __builtin_amdgcn_permlane16_swap(ow[2 * n2][0], ow[2 * n2 + 1][0], false, false);
        const auto r1 = __builtin_amdgcn_permlane16_swap(ow[2 * n2][1], ow[2 * n2 + 1][1], false, false);
        *(u32x4*)(out + (size_t)row * D_ + ct * 64 + n2 * 32 + (fq & 1) * 16 + (fq >> 1) * 8) = (u32x4){r0[0], r1[0], r0[1], r1[1]};
      }
    }
    STAMP_AT(7);
  } else {
    const float dv = bflo(dvraw);
    u32x4 qr[4], gx[2][2], gwv[2];
#pragma unroll
    for (int i = 0; i < 4; ++i) qr[i] = *(const u32x4*)(q + (size_t)(rt * 128 + wid * 32 + i * 8 + (lane >> 3)) * D_ + ct * 64 + (lane & 7) * 8);
#pragma unroll
    for (int r = 0; r < 2; ++r) {
      const u16* xr = q + (size_t)(b * 8 + wid * 2 + r) * D_ + lane * 8;
      gx[r][0] = *(const u32x4*)xr; gx[r][1] = *(const u32x4*)(xr + 512);
    }
    gwv[0] = *(const u32x4*)(gw + lane * 8); gwv[1] = *(const u32x4*)(gw + 512 + lane * 8);
    SCHED();
    STAMP_AT(4);
    conv_wave(lds + wid * CV_SIZE, in, dv, rws, ch);
    {
      float sg2[2];
#pragma unroll
      for (int r = 0; r < 2; ++r) {
        float s = 0.f;
#pragma unroll
        for (int e = 0; e < 4; ++e) {
          s += rbf(bflo(gx[r][0][e]) * bflo(gwv[0][e])) + rbf(bfhi(gx[r][0][e]) * bfhi(gwv[0][e]));
          s += rbf(bflo(gx[r][1][e]) * bflo(gwv[1][e])) + rbf(bfhi(gx[r][1][e]) * bfhi(gwv[1][e]));
        }
#pragma unroll
        for (int o = 32; o >= 1; o >>= 1) s += __shfl_xor(s, o);
        sg2[r] = s;
      }
      const float s = rbf((lane & 1) ? sg2[1] : sg2[0]);
      const float g = rbf(1.0f / (1.0f + __expf(-s)));
      __builtin_amdgcn_raw_buffer_store_b32(__float_as_uint(g), rws, WS_G + (b * 8 + wid * 2 + (lane & 1)) * 4, 0, SC1);
    }
    WAIT_V(0);
    flag_set(ws, WS_FLAG + ((lane & 16) ? (unsigned)((b >> 4) * 16 + (lane & 15)) * FL_STRIDE + FL_G + ((b & 15) * 4 + wid) * 8
                                        : (unsigned)((lane & 15) * 16 + (ch >> 6)) * FL_STRIDE + FL_Z + (ch & 63) * 8), tag);
    STAMP_AT(5);
    *(volatile LAS unsigned*)(lds + L_CTRL + 32 + wid * 4) = 1u;
    LAS char* sg = lds + wid * CV_SIZE + L_STG;
    LAS char* zs = lds + wid * CV_SIZE + L_ZS;
#pragma unroll
    for (int i = 0; i < 4; ++i) *(LAS u32x4*)(sg + (i * 8 + (lane >> 3)) * 144 + (lane & 7) * 16) = qr[i];
    WAIT_L(0);
    u32x2 qv[2][4];
#pragma unroll
    for (int i2 = 0; i2 < 2; ++i2)
#pragma unroll
      for (int n = 0; n < 4; ++n) qv[i2][n] = *(LAS u32x2*)(sg + (i2 * 16 + fr) * 144 + n * 32 + fq * 8);
    lds_wait4<16>(lds + L_CTRL + 48);
    {
      const unsigned tl = (unsigned)tag, th = (unsigned)(tag >> 32);
      for (unsigned spins = 0; spins < (1u << 16); ++spins) {
        const u32x4 v = ld16(rws, lane * 16, myfl + FL_G);
        if (__all(v[0] == tl && v[1] == th && v[2] == tl && v[3] == th)) break;
        __builtin_amdgcn_s_sleep(16);
      }
      asm volatile("" ::: "memory");
      __builtin_amdgcn_fence(__ATOMIC_ACQUIRE, "wavefront");
    }
    float gv[2];
    {
      u32x4 zr[4];
#pragma unroll
      for (int i2 = 0; i2 < 2; ++i2) gv[i2] = __uint_as_float(__builtin_amdgcn_raw_buffer_load_b32(rws, (rt * 128 + (wid * 2 + i2) * 16 + fr) * 4, WS_G, SC1));
#pragma unroll
      for (int i = 0; i < 4; ++i) zr[i] = ld16(rws, (unsigned)(ct * 64 + i * 16 + (lane >> 2)) * 4096u + (lane & 3) * 16, WS_ZT + (unsigned)(rt * 128 + wid * 32) * 2u);
#pragma unroll
      for (int i = 0; i < 4; ++i) *(LAS u32x4*)(zs + (i * 16 + (lane >> 2)) * 80 + (lane & 3) * 16) = zr[i];
    }
    WAIT_L(0);
    const int q4 = (lane & 15) >> 2, p = lane & 3;
#pragma unroll
    for (int i2 = 0; i2 < 2; ++i2) {
      const float g = gv[i2];
#pragma unroll
      for (int n = 0; n < 4; ++n) {
        const s16x4 zz = __builtin_amdgcn_ds_read_tr16_b64_v4i16((LAS s16x4*)(zs + (16 * n + 4 * fq + q4) * 80 + (i2 * 16 + 4 * p) * 2));
        const u32x2 zu = __builtin_bit_cast(u32x2, zz);
        const unsigned q01 = qv[i2][n][0], q23 = qv[i2][n][1];
        f32x4 ga;
        ga[0] = rbf(g * rbf(bflo(q01) * bflo(zu[0]))); ga[1] = rbf(g * rbf(bfhi(q01) * bfhi(zu[0])));
        ga[2] = rbf(g * rbf(bflo(q23) * bflo(zu[1]))); ga[3] = rbf(g * rbf(bfhi(q23) * bfhi(zu[1])));
        *(LAS f32x4*)(sg + (i2 * 4 + n) * 1024 + lane * 16) = ga;
      }
      *(LAS float*)(sg + 8192 + i2 * 256 + lane * 4) = rbf(1.0f - g);
    }
    WAIT_L(0);
    STAMP_AT(6);
    BAR();
    if (wid < 3) *(u32x4*)(ws + myfl + (wid * 64 + lane) * 16) = (u32x4){0u, 0u, 0u, 0u};
    STAMP_AT(7);
  }
}

extern "C" void kernel_launch(void* const* d_in, const int* in_sizes, int n_in, void* d_out, int out_size, void* d_ws, size_t ws_size,
                              hipStream_t stream) {
  const u16* q = (const u16*)d_in[0]; const u16* kk = (const u16*)d_in[1]; const u16* vv = (const u16*)d_in[2];
  const u16* shk = (const u16*)d_in[3]; const u16* ssmk = (const u16*)d_in[4]; const u16* ssmD = (const u16*)d_in[5];
  const u16* wg = (const u16*)d_in[7]; const u16* wu = (const u16*)d_in[8]; const u16* wd = (const u16*)d_in[9];
  const u16* gw = (const u16*)d_in[10];
  constexpr int LDS1 = 163840;
  (void)hipFuncSetAttribute((const void*)k1_kernel, hipFuncAttributeMaxDynamicSharedMemorySize, LDS1);
  k1_kernel<<<256, 512, LDS1, stream>>>(q, kk, vv, shk, ssmk, ssmD, wg, wu, wd, gw, (char*)d_ws, (u16*)d_out);
}
```

```cpp
#define OUT_AUX 2
#include <hip/hip_runtime.h>
#include <hip/hip_bf16.h>

typedef unsigned short u16;
typedef unsigned long long u64;
typedef short s16x4 __attribute__((ext_vector_type(4)));
typedef short s16x8 __attribute__((ext_vector_type(8)));
typedef __bf16 bfx2 __attribute__((ext_vector_type(2)));
typedef float f32x4 __attribute__((ext_vector_type(4)));
typedef unsigned u32x4 __attribute__((ext_vector_type(4)));
typedef unsigned u32x2 __attribute__((ext_vector_type(2)));
typedef __amdgpu_buffer_rsrc_t rsrc_t;
#define LAS __attribute__((address_space(3)))
#define GAS __attribute__((address_space(1)))
#define DEV __device__ __forceinline__
#define AI __attribute__((always_inline))
extern "C" __device__ u64 hw_dispatch_id() __asm("llvm.amdgcn.dispatch.id");

#define G2_N0 12
constexpr int S_ = 2048, D_ = 1024, F_ = 3584;
constexpr unsigned WS_HF = 0;
constexpr unsigned HF_KS = 129 * 1024;
constexpr unsigned WS_KVT = 16u << 20;
constexpr unsigned WS_ZT = 21u << 20;
constexpr unsigned WS_G = 26u << 20;
constexpr unsigned WS_FLAG = 27u << 20;
constexpr unsigned WS_BYTES = 28u << 20;
__device__ constexpr unsigned ch_off(unsigned ch) { return (ch + (ch >> 6)) * 4096u; }
constexpr int FL_H = 0, FL_G = 1024, FL_Z = 1536, FL_KV = 2048, FL_BYTES = 2560;
constexpr unsigned FL_STRIDE = 4096;
constexpr int SC1 = 16;
constexpr int L_PFS = 163840 - 512, L_CTRL = 163840 - 64;

DEV float bflo(unsigned w) { return __uint_as_float(w << 16); }
DEV float bfhi(unsigned w) { return __uint_as_float(w & 0xffff0000u); }
DEV unsigned pack2(float lo, float hi) { bfx2 p; p[0] = (__bf16)lo; p[1] = (__bf16)hi; return __builtin_bit_cast(unsigned, p); }
DEV float rbf(float f) { return (float)(__bf16)f; }
DEV void mfma16a(f32x4& c, s16x8 a, s16x8 b) {
  asm volatile("v_mfma_f32_16x16x32_bf16 %0, %1, %2, %0" : "+v"(c) : "v"(a), "v"(b));
}
DEV void mfma16z(f32x4& c, s16x8 a, s16x8 b) {
  asm volatile("v_mfma_f32_16x16x32_bf16 %0, %1, %2, 0" : "=&v"(c) : "v"(a), "v"(b));
}
DEV s16x8 tr_read8(LAS char* p0, LAS char* p1) {
  s16x4 lo = __builtin_amdgcn_ds_read_tr16_b64_v4i16((LAS s16x4*)p0);
  s16x4 hi = __builtin_amdgcn_ds_read_tr16_b64_v4i16((LAS s16x4*)p1);
  return __builtin_shufflevector(lo, hi, 0, 1, 2, 3, 4, 5, 6, 7);
}
template <int OFF, int D2>
DEV s16x8 tr_read8_asm(unsigned addr) {
  s16x4 lo, hi;
  asm volatile("ds_read_b64_tr_b16 %0, %2 offset:%3\n\tds_read_b64_tr_b16 %1, %2 offset:%4" : "=&v"(lo), "=&v"(hi) : "v"(addr), "i"(OFF), "i"(OFF + D2));
  return __builtin_shufflevector(lo, hi, 0, 1, 2, 3, 4, 5, 6, 7);
}
template <int OFF>
DEV s16x8 ds_read16_asm(unsigned addr) {
  s16x8 r;
  asm volatile("ds_read_b128 %0, %1 offset:%2" : "=v"(r) : "v"(addr), "i"(OFF));
  return r;
}
template <int V> struct IC { static constexpr int value = V; };
DEV rsrc_t mk_rsrc(const void* p, unsigned bytes) { return __builtin_amdgcn_make_buffer_rsrc((void*)p, 0, bytes, 0x00020000); }
DEV void blds16(rsrc_t r, LAS char* l, unsigned voff, unsigned soff) {
  __builtin_amdgcn_raw_ptr_buffer_load_lds(r, (LAS void*)l, 16, voff, soff, 0, 0);
}
DEV void blds4(rsrc_t r, LAS char* l, unsigned voff, unsigned soff) {
  __builtin_amdgcn_raw_ptr_buffer_load_lds(r, (LAS void*)l, 4, voff, soff, 0, 0);
}
DEV u32x4 ld16(rsrc_t r, unsigned voff, unsigned soff) { return __builtin_amdgcn_raw_buffer_load_b128(r, voff, soff, SC1); }
DEV void st16(rsrc_t r, unsigned voff, u32x4 v) { __builtin_amdgcn_raw_buffer_store_b128(v, r, voff, 0, SC1); }
#define STAMP_AT(i)
#define WAIT_V(n) asm volatile("s_waitcnt vmcnt(" #n ")" ::: "memory")
#define WAIT_L(n) asm volatile("s_waitcnt lgkmcnt(" #n ")" ::: "memory")
#define BAR() __builtin_amdgcn_s_barrier()
#define SCHED() __builtin_amdgcn_sched_barrier(0)
#define MFMA_DRAIN() do { asm volatile("s_nop 7\n\ts_nop 7" ::: "memory"); SCHED(); } while (0)

DEV void flag_set(char* ws, unsigned off, u64 tag) { __hip_atomic_store((GAS u64*)(ws + off), tag, __ATOMIC_RELAXED, __HIP_MEMORY_SCOPE_AGENT); }
DEV u64 flag_peek(char* ws, unsigned off, int n) {
  const int lane = threadIdx.x & 63;
  return __hip_atomic_load((GAS u64*)(ws + off + 8 * (lane < n ? lane : 0)), __ATOMIC_RELAXED, __HIP_MEMORY_SCOPE_AGENT);
}
template <int SLP>
DEV void flag_wait(u64 first, char* ws, unsigned off, int n, u64 tag) {
  u64 v = first;
  for (unsigned spins = 0; spins < (1u << 16); ++spins) {
    if (__all(v == tag)) break;
    __builtin_amdgcn_s_sleep(SLP);
    v = flag_peek(ws, off, n);
  }
  __builtin_amdgcn_fence(__ATOMIC_ACQUIRE, "wavefront");
}
template <int SLP = 1>
DEV void lds_wait4(LAS char* p) {
  for (unsigned spins = 0; spins < (1u << 22); ++spins) {
    const u32x4 dn = *(volatile LAS u32x4*)p;
    if (__builtin_amdgcn_readfirstlane(dn[0] & dn[1] & dn[2] & dn[3]) != 0u) break;
    __builtin_amdgcn_s_sleep(SLP);
  }
  asm volatile("" ::: "memory");
}
DEV int tile_of_block(int b) { const int xcd = b & 7, j = b >> 3; return ((xcd >> 1) * 4 + (j >> 3)) * 16 + (xcd & 1) * 8 + (j & 7); }

struct Args {
  const u16 *q, *kk, *vv, *shk, *ssmk, *ssmD, *wg, *wu, *wd, *gw;
  char* ws; u16* out;
};

DEV void gemm1_main(f32x4 (&acc)[4][8], u64& kvpeek, unsigned kvoff, LAS char* lds, const Args& a, int pm, int pn, int im, int in_) {
  char* ws = a.ws;
  const int tid = threadIdx.x, lane = tid & 63;
  const int wid = __builtin_amdgcn_readfirstlane(tid >> 6);
  const int wr = wid >> 1, wc = wid & 1;
  const int fr = lane & 15, fq = lane >> 4;
  unsigned va[2], vb[2];
#pragma unroll
  for (int e = 0; e < 2; ++e) {
    const int rl = lane >> 3, p = lane & 7;
    const int sw = (e << 2) | (lane >> 4);
    va[e] = (unsigned)(rl * 2048 + ((p ^ sw) * 16));
    const int kl = lane >> 4, pb = lane & 15;
    const int swb = (kl | (e << 2)) << 1;
    vb[e] = (unsigned)(kl * (F_ * 2) + (pb ^ swb) * 16);
  }
  const rsrc_t rx = mk_rsrc(a.q, S_ * D_ * 2);
  const rsrc_t rw = mk_rsrc((wid < 4) ? a.wg : a.wu, D_ * F_ * 2);
  const unsigned xa_base = (unsigned)(pm * 256 + wid * 32) * 2048u;
  const unsigned wb_base = (unsigned)((wid & 3) * 16) * (F_ * 2) + pn * 256;
  LAS char* ldsA = lds + wid * 4096;
  LAS char* ldsB = lds + 65536 + wid * 4096;
  auto dma = [&](int kt, int st, int d) AI {
    const int i = d >> 1;
    if (d & 1) blds16(rw, ldsB + st * 32768 + i * 1024, vb[i >> 1], wb_base + (kt * 64 + i * 4) * (F_ * 2));
    else blds16(rx, ldsA + st * 32768 + i * 1024, va[i & 1], xa_base + i * (8 * 2048) + kt * 128);
  };
#pragma unroll
  for (int d = 0; d < 8; ++d) dma(0, 0, d);
#pragma unroll
  for (int d = 0; d < 8; ++d) dma(1, 1, d);
  SCHED();
  rsrc_t rp; unsigned vp, pstep;
  if (wid < 4) {
    rp = mk_rsrc((im >> 1) ? a.wu : a.wg, D_ * F_ * 2);
    vp = (unsigned)(wid * 16 + (lane & 15)) * (F_ * 2) + pn * 256 + (im & 1) * 128; pstep = 64 * F_ * 2;
  } else {
    rp = rx;
    int r = in_ * 37 + (wid - 4) * 10 + ((lane & 15) < 10 ? (lane & 15) : 9); r = r > 255 ? 255 : r;
    vp = (unsigned)(pm * 256 + r) * 2048u; pstep = 128;
  }
  vp += (unsigned)(lane >> 4) * 32u;
  auto pf = [&](int kt) AI { blds4(rp, lds + L_PFS, vp, kt * pstep); };
  const unsigned lds0 = (unsigned)(size_t)lds;
  unsigned bx[2];
#pragma unroll
  for (int ks = 0; ks < 2; ++ks) bx[ks] = lds0 + (wr * 64 + fr) * 128 + (((ks * 4 + fq) ^ (fr >> 1)) * 16);
  unsigned bw[4];
  {
    const int q = (lane & 15) >> 2, p = lane & 3;
    const int sw = (q | ((fq & 1) << 2)) << 1;
#pragma unroll
    for (int j = 0; j < 4; ++j) bw[j] = lds0 + 65536 + (8 * fq + q) * 256 + (((wc * 8 + j * 2 + (p >> 1)) ^ sw) * 16) + (p & 1) * 8;
  }
  auto ldx = [&](auto ST, auto KS, const int i) AI -> s16x8 {
    constexpr int o = decltype(ST)::value * 32768; const unsigned p = bx[decltype(KS)::value];
    return i == 0 ? ds_read16_asm<o>(p) : i == 1 ? ds_read16_asm<o + 2048>(p) : i == 2 ? ds_read16_asm<o + 4096>(p) : ds_read16_asm<o + 6144>(p);
  };
  auto ldw = [&](auto ST, auto KS, const int n) AI -> s16x8 {
    constexpr int o = decltype(ST)::value * 32768 + decltype(KS)::value * 8192;
    return n < 4 ? tr_read8_asm<o, 1024>(bw[n & 3]) : tr_read8_asm<o + 16384, 1024>(bw[n & 3]);
  };
  s16x8 xa[4], xb[4], w[8];
  WAIT_V(8); BAR(); SCHED();
  STAMP_AT(1);
#pragma unroll
  for (int i = 0; i < 4; ++i) xa[i] = ldx(IC<0>{}, IC<0>{}, i);
#pragma unroll
  for (int n = 0; n < 8; ++n) w[n] = ldw(IC<0>{}, IC<0>{}, n);
  pf(2); pf(3); pf(4);
  WAIT_L(0);
  SCHED();

  auto tile_body = [&](auto ST, const int kt, const bool pre1, const bool pre2, const int hook) AI {
    constexpr int st = decltype(ST)::value;
#pragma unroll
    for (int n = 0; n < 8; ++n) {
      if (pre2) { if (n == 2) __builtin_amdgcn_s_setprio(1); if (n == 6) __builtin_amdgcn_s_setprio(0); }
      if (n == 6) WAIT_L(15);
#pragma unroll
      for (int i = 0; i < 4; ++i) {
        if (n == 0) { if (i == 0) WAIT_L(13); if (i == 1) WAIT_L(10); if (i == 2) WAIT_L(7); if (i == 3) WAIT_L(4); }
        if (hook == 1) mfma16z(acc[i][n], w[n], xa[i]); else mfma16a(acc[i][n], w[n], xa[i]);
      }
      w[n] = ldw(ST, IC<1>{}, n);
      if (n < 4) xb[n] = ldx(ST, IC<1>{}, n);
      if (n == 3 && hook == 0) pf(kt + 4);
      if (n == 3 && hook == 5) kvpeek = flag_peek(ws, kvoff, 32);
      SCHED();
    }
#pragma unroll
    for (int n = 0; n < 2; ++n) {
#pragma unroll
      for (int i = 0; i < 4; ++i) {
        if (n == 0) { if (i == 0) WAIT_L(15); if (i == 1) WAIT_L(14); if (i == 2) WAIT_L(11); if (i == 3) WAIT_L(8); }
        mfma16a(acc[i][n], w[n], xb[i]);
      }
    }
    SCHED();
    WAIT_L(0);
    if (pre1) {
      if (hook == 1) { WAIT_V(3); } else if (hook == 7) { WAIT_V(0); } else { WAIT_V(1); }
      BAR(); SCHED();
      if (pre2) __builtin_amdgcn_s_setprio(3);
      else if (wid < 4) __builtin_amdgcn_s_setprio(2);
      else __builtin_amdgcn_s_setprio(1);
      w[0] = ldw(IC<(st ^ 1)>{}, IC<0>{}, 0); w[1] = ldw(IC<(st ^ 1)>{}, IC<0>{}, 1);
      if (pre2) { dma(kt + 2, st, 0); dma(kt + 2, st, 1); }
      SCHED();
    }
#pragma unroll
    for (int n = 2; n < 8; ++n) {
      if (pre2 && n == 6) __builtin_amdgcn_s_setprio(2);
#pragma unroll
      for (int i = 0; i < 4; ++i) mfma16a(acc[i][n], w[n], xb[i]);
      if (pre1) {
        w[n] = ldw(IC<(st ^ 1)>{}, IC<0>{}, n);
        if (n - 2 < 4) xa[n - 2] = ldx(IC<(st ^ 1)>{}, IC<0>{}, n - 2);
        if (pre2) dma(kt + 2, st, n);
      }
      SCHED();
    }
  };
  tile_body(IC<0>{}, 0, true, true, 1);
  tile_body(IC<1>{}, 1, true, true, 0);
  for (int kt = 2; kt < 12; kt += 2) {
    tile_body(IC<0>{}, kt, true, true, 0);
    tile_body(IC<1>{}, kt + 1, true, true, 0);
  }
  tile_body(IC<0>{}, 12, true, true, 5);
  tile_body(IC<1>{}, 13, true, true, 7);
  tile_body(IC<0>{}, 14, true, false, 7);
  tile_body(IC<1>{}, 15, false, false, 7);
  *(volatile LAS unsigned*)(lds + L_CTRL + wid * 4) = 1u;
  MFMA_DRAIN();
  STAMP_AT(2);
}

constexpr unsigned CODE_BYTES = 44 * 1024;
DEV void light_unit(LAS char* lds, const Args& a, rsrc_t rws, u64 tag, int cu, const void* codebase) {
  char* ws = a.ws;
  const int tid = threadIdx.x, lane = tid & 63;
  const int wid = __builtin_amdgcn_readfirstlane(tid >> 6);
  const int cb = cu & 7, tblk = (cu >> 3) * 8 + wid;
  const int kch = cb * 128 + lane * 2;
  LAS char* wl = lds + wid * (128 * 144);
  const u32x4 shw = *(const u32x4*)(a.shk + kch * 4);
#pragma unroll
  for (int hf = 0; hf < 2; ++hf) {
    const int t0 = tblk * 64 + hf * 32;
    unsigned kr[35], vr[32];
#pragma unroll
    for (int jx = 0; jx < 35; ++jx) { const int t = t0 - 3 + jx; kr[jx] = *(const unsigned*)(a.kk + (size_t)(t < 0 ? 0 : t) * D_ + kch); }
#pragma unroll
    for (int jx = 0; jx < 32; ++jx) vr[jx] = *(const unsigned*)(a.vv + (size_t)(t0 + jx) * D_ + kch);
    SCHED();
#pragma unroll
    for (int c = 0; c < 2; ++c) {
      const unsigned s01 = shw[c * 2], s23 = shw[c * 2 + 1];
      const float w0 = bflo(s01), w1 = bfhi(s01), w2 = bflo(s23), w3 = bfhi(s23);
#pragma unroll
      for (int sb = 0; sb < 4; ++sb) {
        float kf[11];
#pragma unroll
        for (int jx = 0; jx < 11; ++jx) { kf[jx] = c ? bfhi(kr[sb * 8 + jx]) : bflo(kr[sb * 8 + jx]); if (sb == 0 && jx < 3 && t0 == 0) kf[jx] = 0.f; }
        float o[8];
#pragma unroll
        for (int jx = 0; jx < 8; ++jx) o[jx] = (kf[jx] * w3 + kf[jx + 1] * w2 + kf[jx + 2] * w1 + kf[jx + 3] * w0) * (c ? bfhi(vr[sb * 8 + jx]) : bflo(vr[sb * 8 + jx]));
        *(LAS u32x4*)(wl + (lane * 2 + c) * 144 + (hf * 4 + sb) * 16) = (u32x4){pack2(o[7], o[6]), pack2(o[5], o[4]), pack2(o[3], o[2]), pack2(o[1], o[0])};
      }
    }
  }
  WAIT_L(0);
#pragma unroll
  for (int e = 0; e < 16; ++e) {
    const int c = e * 8 + (lane >> 3);
    const u32x4 v = *(LAS u32x4*)(wl + c * 144 + (lane & 7) * 16);
    st16(rws, WS_KVT + ch_off(cb * 128 + c) + tblk * 128 + (lane & 7) * 16, v);
  }
  WAIT_V(0);
  flag_set(ws, WS_FLAG + (unsigned)tile_of_block(cb * 32 + (lane & 31)) * FL_STRIDE + FL_KV + tblk * 8, tag);
  STAMP_AT(1);
  const rsrc_t rd = mk_rsrc(a.wd, F_ * D_ * 2), rk = mk_rsrc(a.ssmk, D_ * S_ * 2);
#pragma unroll
  for (int i = 0; i < 2; ++i) blds4(rk, lds + L_PFS, tid * 128, cu * 131072 + i * 65536);
#pragma unroll
  for (int i = 0; i < 4; ++i) blds4(rd, lds + L_PFS, tid * 128, (i * 32 + cu) * 57344);
  WAIT_V(0);
  STAMP_AT(2);
  __syncthreads();
}

DEV u32x2 geglu4(f32x4 gt, f32x4 up) {
  const float C1 = -2.302208198f, C2 = -2.302208198f * 0.044715f;
  const f32x4 wv = gt * (gt * gt * C2 + C1);
  f32x4 ev;
#pragma unroll
  for (int r = 0; r < 4; ++r) ev[r] = __builtin_amdgcn_exp2f(wv[r]);
  const f32x4 dv = ev + 1.0f;
  f32x4 rv;
#pragma unroll
  for (int r = 0; r < 4; ++r) rv[r] = __builtin_amdgcn_rcpf(dv[r]);
  const f32x4 hv = gt * up * rv;
  return (u32x2){pack2(hv[0], hv[1]), pack2(hv[2], hv[3])};
}
DEV void gemm1_epi(const f32x4 (&acc)[4][8], rsrc_t rws, int pm, int pn) {
  const int lane = threadIdx.x & 63;
  const int wid = __builtin_amdgcn_readfirstlane(threadIdx.x >> 6);
  const int wr = wid >> 1, wc = wid & 1;
  const unsigned off = WS_HF + (unsigned)(pn * 4 + wc * 2) * HF_KS + (unsigned)(pm * 16 + wr * 4) * 1024u + lane * 16;
#pragma unroll
  for (int i = 0; i < 4; ++i)
#pragma unroll
    for (int k2 = 0; k2 < 2; ++k2) {
      const u32x2 h0 = geglu4(acc[i][k2 * 2], acc[i][4 + k2 * 2]), h1 = geglu4(acc[i][k2 * 2 + 1], acc[i][4 + k2 * 2 + 1]);
      st16(rws, off + k2 * HF_KS + i * 1024, (u32x4){h0[0], h0[1], h1[0], h1[1]});
    }
}

constexpr int CV_CS = 4160, CV_U = 4 * CV_CS  , CV_SIZE = 23552;
struct ConvIn { u32x4 own[4]; u32x2 nxt[4], h0; u32x4 u[4]; };

DEV void conv_write_seg(LAS char* wl, int seg, u32x4 a, u32x2 b) {
  const unsigned a01 = __builtin_amdgcn_alignbit(a[1], a[0], 16), a12 = __builtin_amdgcn_alignbit(a[2], a[1], 16),
                 a23 = __builtin_amdgcn_alignbit(a[3], a[2], 16), a34 = __builtin_amdgcn_alignbit(b[0], a[3], 16),
                 a45 = __builtin_amdgcn_alignbit(b[1], b[0], 16);
  LAS char* dst = wl + seg * 16;
  *(LAS u32x4*)(dst) = a;
  *(LAS u32x4*)(dst + CV_CS) = (u32x4){a01, a12, a23, a34};
  *(LAS u32x4*)(dst + 2 * CV_CS) = (u32x4){a[1], a[2], a[3], b[0]};
  *(LAS u32x4*)(dst + 3 * CV_CS) = (u32x4){a12, a23, a34, a45};
}
DEV s16x8 conv_ldp(LAS char* p) {
  volatile LAS s16x4* pp = (volatile LAS s16x4*)p;
  const s16x4 lo = pp[0], hi = pp[1];
  return __builtin_shufflevector(lo, hi, 0, 1, 2, 3, 4, 5, 6, 7);
}

DEV void conv_wave(LAS char* wl, const ConvIn& in, float dv, rsrc_t rws, int ch) {
  const int lane = threadIdx.x & 63;
  const int fr = lane & 15, fq = lane >> 4;
  const u32x4 zero4 = (u32x4){0u, 0u, 0u, 0u};
#pragma unroll
  for (int p = 0; p < 4; ++p) conv_write_seg(wl, 64 * p + lane + 2, in.own[p], in.nxt[p]);
  conv_write_seg(wl, lane & 1, zero4, (lane & 1) ? in.h0 : (u32x2){0u, 0u});
  *(LAS u32x4*)(wl + CV_U + (lane & 31) * 16) = zero4;
#pragma unroll
  for (int i = 0; i < 4; ++i) *(LAS u32x4*)(wl + CV_U + 512 + i * 1024 + lane * 16) = in.u[i];
  const int o_ = 1 + fr + 8 * fq;
  LAS char* pb = wl + (o_ & 3) * CV_CS + 2 * (o_ & ~3);
  LAS char* qb = wl + CV_U + 16 * (33 + 2 * fr - fq);
  f32x4 acc[8];
#pragma unroll
  for (int g = 0; g < 8; ++g) acc[g] = (f32x4){0.f, 0.f, 0.f, 0.f};
  s16x8 P0[8], Q0[8], P1[8], Q1[8];
  WAIT_L(0); SCHED();
#pragma unroll
  for (int a = 0; a < 8; ++a) { P0[a] = conv_ldp(pb + a * 512); Q0[a] = *(LAS s16x8*)(qb + a * 512); }
  SCHED();
  auto cls = [&](s16x8 (&P)[8], s16x8 (&Q)[8], s16x8 (&Pn)[8], s16x8 (&Qn)[8], LAS char* pn, LAS char* qn) AI {
#pragma unroll
    for (int b = 0; b < 8; ++b) {
      Pn[b] = conv_ldp(pn + b * 512); Qn[b] = *(LAS s16x8*)(qn + b * 512);
#pragma unroll
      for (int a = 0; a + b < 8; ++a) mfma16a(acc[a + b], P[a], Q[b]);
      SCHED();
    }
  };
  for (int r = 0; r < 8; r += 2) {
    cls(P0, Q0, P1, Q1, pb + (r + 1) * 64, qb - (r + 1) * 64);
    const int rn = (r + 2) & 7;
    cls(P1, Q1, P0, Q0, pb + rn * 64, qb - rn * 64);
  }
  MFMA_DRAIN();
#pragma unroll
  for (int e = 0; e < 4; ++e) {
    unsigned zx[2][2];
#pragma unroll
    for (int h = 0; h < 2; ++h) {
      const int g = 2 * e + h;
      const int al = 32 * g + 2 * fr + (fq >> 1);
      const u32x2 kvp = *(LAS u32x2*)(wl + CV_U + 512 + al * 16 + ((fq & 1) ? 0 : 8));
      const float k3 = bflo(kvp[0]), k2 = bfhi(kvp[0]), k1 = bflo(kvp[1]), k0 = bfhi(kvp[1]);
      const float z0 = rbf(acc[g][0]) + rbf(k0 * dv), z1 = rbf(acc[g][1]) + rbf(k1 * dv), z2 = rbf(acc[g][2]) + rbf(k2 * dv), z3 = rbf(acc[g][3]) + rbf(k3 * dv);
      zx[h][0] = pack2(z0, z1); zx[h][1] = pack2(z2, z3);
    }
    const auto r0 = __builtin_amdgcn_permlane16_swap(zx[0][0], zx[1][0], false, false);
    const auto r1 = __builtin_amdgcn_permlane16_swap(zx[0][1], zx[1][1], false, false);
    st16(rws, WS_ZT + ch_off(ch) + (unsigned)(256 * (2 * e + (fq & 1)) + 16 * fr + 8 * (fq >> 1)) * 2u, (u32x4){r0[0], r1[0], r0[1], r1[1]});
  }
}

constexpr int L_STG = 8192, L_ZS = L_STG + 9216, L_RING = 4 * CV_SIZE;
static_assert(L_ZS + 5120 <= CV_SIZE && CV_U + 4608 <= CV_SIZE && L_RING + 65536 <= L_PFS, "lds map");

__global__ __launch_bounds__(512, 2) void k1_kernel(const u16* __restrict__ q, const u16* __restrict__ kk, const u16* __restrict__ vv,
                                                    const u16* __restrict__ shk, const u16* __restrict__ ssmk, const u16* __restrict__ ssmD,
                                                    const u16* __restrict__ wg, const u16* __restrict__ wu, const u16* __restrict__ wd,
                                                    const u16* __restrict__ gw, char* ws, u16* __restrict__ out) {
  extern __shared__ __attribute__((aligned(16))) char shm[];
  LAS char* lds = (LAS char*)shm;
  const void* codebase = (const void*)(__builtin_amdgcn_s_getpc() & ~255ull);
  const int tid = threadIdx.x, lane = tid & 63;
  const int wid = __builtin_amdgcn_readfirstlane(tid >> 6);
  const int fr = lane & 15, fq = lane >> 4;
  const int b = blockIdx.x, xcd = b & 7, j = b >> 3;
  const u64 tag = ((hw_dispatch_id() * 0x9E3779B97F4A7C15ull) ^ 0xD1B54A32D192ED03ull) | 1ull;
  const rsrc_t rws = mk_rsrc(ws, WS_BYTES);
  const int rt = (xcd >> 1) * 4 + (j >> 3), ct = (xcd & 1) * 8 + (j & 7);
  const unsigned myfl = WS_FLAG + (unsigned)(rt * 16 + ct) * FL_STRIDE;
  const int ch = b * 4 + (wid & 3);
  const bool light = j < 4;
  const int jj = light ? 0 : j - 4;
  const int pm = (xcd & 1) * 4 + (jj & 3), pn = (xcd >> 1) * 7 + (jj >> 2);
  const Args a{q, kk, vv, shk, ssmk, ssmD, wg, wu, wd, gw, ws, out};
  STAMP_AT(0);

  if (tid < 16) *(volatile LAS unsigned*)(lds + L_CTRL + tid * 4) = (light && tid < 8) ? 1u : 0u;
  ConvIn in;
  const u16* hrow = ssmk + (size_t)ch * S_;
  unsigned dvraw = 0;
  auto conv_issue_h = [&]() AI {
#pragma unroll
    for (int p = 0; p < 4; ++p) {
      in.own[p] = *(const u32x4*)(hrow + 8 * (64 * p + lane));
      const int nx = 64 * p + lane + 1;
      in.nxt[p] = *(const u32x2*)(hrow + 8 * (nx < 256 ? nx : 255));
    }
    in.h0 = *(const u32x2*)hrow;
    dvraw = ssmD[ch];
  };
  auto conv_issue_u = [&]() AI {
#pragma unroll
    for (int i = 0; i < 4; ++i) in.u[i] = ld16(rws, lane * 16 + i * 1024, WS_KVT + ch_off(ch));
  };
  u64 kv0 = 0;
  if (light) {
    light_unit(lds, a, rws, tag, xcd * 4 + j, codebase);
    conv_issue_h();
    flag_wait<4>(kv0, ws, myfl + FL_KV, 32, tag);
    conv_issue_u();
  } else {
    f32x4 acc1[4][8];
    gemm1_main(acc1, kv0, myfl + FL_KV, lds, a, pm, pn, jj & 3, jj >> 2);
    flag_wait<4>(kv0, ws, myfl + FL_KV, 32, tag);
    conv_issue_h();
    conv_issue_u();
    SCHED();
    gemm1_epi(acc1, rws, pm, pn);
    if (wid < 4) __builtin_amdgcn_s_setprio(0);
    WAIT_V(0);
    flag_set(ws, WS_FLAG + (unsigned)((pm * 2 + (wid >> 2)) * 16 + (lane & 15)) * FL_STRIDE + FL_H + (((wid & 1) * 28 + pn) * 2 + ((wid >> 1) & 1)) * 8, tag);
  }
  STAMP_AT(3);
  lds_wait4(lds + L_CTRL); lds_wait4(lds + L_CTRL + 16);

  if (wid >= 4) {
    const int w4 = wid - 4;
    const unsigned lo16 = lane * 16;
    const unsigned ha = WS_HF + (unsigned)w4 * HF_KS + (unsigned)(rt * 8) * 1024u;
    const rsrc_t rd = mk_rsrc(wd, F_ * D_ * 2);
    unsigned vbs[2];
#pragma unroll
    for (int gp = 0; gp < 2; ++gp)
      vbs[gp] = (unsigned)((lane >> 3) & 3) * 2048u + (((lane & 7) ^ ((((lane >> 4) & 1) | (gp << 1)) << 1)) * 16) + (lane >> 5) * (16 * 2048u);
    LAS char* ring = lds + L_RING + w4 * 16384;
    unsigned bb[4];
    {
      const int q4 = (lane & 15) >> 2, p = lane & 3;
      const int f2 = (q4 >> 1) | ((fq & 1) << 1);
#pragma unroll
      for (int n = 0; n < 4; ++n) bb[n] = (unsigned)(size_t)ring + (8 * fq + q4) * 128 + ((((n ^ f2) << 1) + (p >> 1)) * 16) + (p & 1) * 8;
    }
    s16x8 A[2][8], B[2][4];
    auto ldA = [&](int s, int i) AI { return __builtin_bit_cast(s16x8, ld16(rws, lo16 + (i & 3) * 1024, ha + (unsigned)s * (4 * HF_KS) + (i >> 2) * 4096)); };
    auto dmaB = [&](int s, int slot) AI {
#pragma unroll
      for (int g = 0; g < 4; ++g) blds16(rd, ring + slot * 4096 + g * 1024, vbs[g & 1], (unsigned)((4 * s + w4) * 32 + 4 * g) * 2048u + ct * 128);
    };
    auto ldB = [&](const int slot, int n) AI {
      return slot == 0 ? tr_read8_asm<0, 512>(bb[n]) : slot == 1 ? tr_read8_asm<4096, 512>(bb[n]) : slot == 2 ? tr_read8_asm<8192, 512>(bb[n]) : tr_read8_asm<12288, 512>(bb[n]);
    };
    const unsigned hfo = myfl + FL_H + (w4 >> 1) * 448;
    u64 rdy = 0, used = 0;
    auto poll_eval = [&](u64 v) AI {
      const u64 m = __ballot(v == tag);
      rdy = m & (m >> 1) & 0x0055555555555555ull;
      __builtin_amdgcn_fence(__ATOMIC_ACQUIRE, "wavefront");
    };
    auto poll_until = [&](const int want) AI {
      for (unsigned spins = 0;; ++spins) {
        poll_eval(flag_peek(ws, hfo, 56));
        if (__builtin_popcountll(rdy & ~used) >= want) break;
        if (spins > (1u << 16)) { rdy = 0x0055555555555555ull; break; }
        __builtin_amdgcn_s_sleep(4);
      }
    };
    auto next = [&]() AI -> int {
      const int bit = __builtin_ctzll(rdy & ~used);
      used |= 1ull << bit;
      return bit >> 1;
    };
    auto need = [&](const int n) AI { if (__builtin_popcountll(rdy & ~used) < n) poll_until(n); };
    poll_until(G2_N0);
    STAMP_AT(4);
    __builtin_amdgcn_s_setprio(3);
    const int o0 = next(), o1 = next();
    int q2 = next(), q3;
    dmaB(o0, 0); dmaB(o1, 1); dmaB(q2, 2);
#pragma unroll
    for (int i = 0; i < 8; ++i) A[0][i] = ldA(o0, i);
#pragma unroll
    for (int i = 0; i < 8; ++i) A[1][i] = ldA(o1, i);
    f32x4 acc[8][4];
    WAIT_V(16);
#pragma unroll
    for (int n = 0; n < 4; ++n) B[0][n] = ldB(0, n);
    SCHED();
    auto step = [&](const int sq, const bool first, const bool pre3, const bool pre2, const bool pre1) AI {
      const int sl = sq & 1;
      if (pre3) q3 = next();
      WAIT_L(0);
      if (pre3) dmaB(q3, (sq + 3) & 3);
      if (pre1) {
        if (pre3) { WAIT_V(24); } else if (pre2) { WAIT_V(20); } else { WAIT_V(8); }
#pragma unroll
        for (int n = 0; n < 4; ++n) B[sl ^ 1][n] = ldB((sq + 1) & 3, n);
      }
      SCHED();
#pragma unroll
      for (int i = 0; i < 8; ++i) {
#pragma unroll
        for (int n = 0; n < 4; ++n) { if (first) mfma16z(acc[i][n], B[sl][n], A[sl][i]); else mfma16a(acc[i][n], B[sl][n], A[sl][i]); }
        if (pre2) A[sl][i] = ldA(q2, i);
        SCHED();
      }
      q2 = q3;
    };
    step(0, true, true, true, true); step(1, false, true, true, true); step(2, false, true, true, true); step(3, false, true, true, true);
    u64 pv = flag_peek(ws, hfo, 56);
    for (int s = 4; s < 24; s += 4) {
      if (s == 8) poll_eval(pv);
      if (s == 12) { STAMP_AT(5); *(volatile LAS unsigned*)(lds + L_CTRL + 48 + w4 * 4) = 1u; }
      need(4);
      step(0, false, true, true, true); step(1, false, true, true, true); step(2, false, true, true, true); step(3, false, true, true, true);
    }
    need(1);
    step(0, false, true, true, true); step(1, false, false, true, true); step(2, false, false, false, true); step(3, false, false, false, false);
    MFMA_DRAIN();
    STAMP_AT(6);
    lds_wait4(lds + L_CTRL + 32);
#pragma unroll
    for (int o = 0; o < 4; ++o) {
      if (o != w4) {
        const int d = (o < w4) ? o : o - 1;
        LAS char* dst = (d < 2) ? ring + d * 8192 : lds + w4 * CV_SIZE;
#pragma unroll
        for (int i2 = 0; i2 < 2; ++i2)
#pragma unroll
          for (int n = 0; n < 4; ++n) *(LAS f32x4*)(dst + (i2 * 4 + n) * 1024 + lo16) = acc[o * 2 + i2][n];
      }
    }
    f32x4 fin[2][4];
#pragma unroll
    for (int o = 0; o < 4; ++o)
      if (o == w4) {
#pragma unroll
        for (int i2 = 0; i2 < 2; ++i2)
#pragma unroll
          for (int n = 0; n < 4; ++n) fin[i2][n] = acc[o * 2 + i2][n];
      }
    WAIT_L(0);
    BAR();
    asm volatile("" ::: "memory");
#pragma unroll
    for (int k = 1; k < 4; ++k) {
      const int sw = (w4 + k) & 3;
      const int d = (w4 < sw) ? w4 : w4 - 1;
      LAS char* src = (d < 2) ? lds + L_RING + sw * 16384 + d * 8192 : lds + sw * CV_SIZE;
#pragma unroll
      for (int i2 = 0; i2 < 2; ++i2)
#pragma unroll
        for (int n = 0; n < 4; ++n) fin[i2][n] += *(LAS f32x4*)(src + (i2 * 4 + n) * 1024 + lo16);
    }
    LAS char* sg = lds + w4 * CV_SIZE + L_STG;
#pragma unroll
    for (int i2 = 0; i2 < 2; ++i2) {
      const int row = rt * 128 + (w4 * 2 + i2) * 16 + fr;
      const float og = *(LAS float*)(sg + 8192 + i2 * 256 + lane * 4);
      unsigned ow[4][2];
#pragma unroll
      for (int n = 0; n < 4; ++n) {
        const f32x4 ga = *(LAS f32x4*)(sg + (i2 * 4 + n) * 1024 + lo16);
        float r4[4];
#pragma unroll
        for (int r = 0; r < 4; ++r) r4[r] = ga[r] + rbf(og * rbf(fin[i2][n][r]));
        ow[n][0] = pack2(r4[0], r4[1]); ow[n][1] = pack2(r4[2], r4[3]);
      }
#pragma unroll
      for (int n2 = 0; n2 < 2; ++n2) {
        const auto r0 = __builtin_amdgcn_permlane16_swap(ow[2 * n2][0], ow[2 * n2 + 1][0], false, false);
        const auto r1 = __builtin_amdgcn_permlane16_swap(ow[2 * n2][1], ow[2 * n2 + 1][1], false, false);
        __builtin_amdgcn_raw_buffer_store_b128((u32x4){r0[0], r1[0], r0[1], r1[1]}, mk_rsrc(out, S_ * D_ * 2), (row * D_ + ct * 64 + n2 * 32 + (fq & 1) * 16 + (fq >> 1) * 8) * 2, 0, OUT_AUX);
      }
    }
    STAMP_AT(7);
  } else {
    const float dv = bflo(dvraw);
    u32x4 qr[4], gx[2][2], gwv[2];
#pragma unroll
    for (int i = 0; i < 4; ++i) qr[i] = *(const u32x4*)(q + (size_t)(rt * 128 + wid * 32 + i * 8 + (lane >> 3)) * D_ + ct * 64 + (lane & 7) * 8);
#pragma unroll
    for (int r = 0; r < 2; ++r) {
      const u16* xr = q + (size_t)(b * 8 + wid * 2 + r) * D_ + lane * 8;
      gx[r][0] = *(const u32x4*)xr; gx[r][1] = *(const u32x4*)(xr + 512);
    }
    gwv[0] = *(const u32x4*)(gw + lane * 8); gwv[1] = *(const u32x4*)(gw + 512 + lane * 8);
    SCHED();
    STAMP_AT(4);
    conv_wave(lds + wid * CV_SIZE, in, dv, rws, ch);
    {
      float sg2[2];
#pragma unroll
      for (int r = 0; r < 2; ++r) {
        float s = 0.f;
#pragma unroll
        for (int e = 0; e < 4; ++e) {
          s += rbf(bflo(gx[r][0][e]) * bflo(gwv[0][e])) + rbf(bfhi(gx[r][0][e]) * bfhi(gwv[0][e]));
          s += rbf(bflo(gx[r][1][e]) * bflo(gwv[1][e])) + rbf(bfhi(gx[r][1][e]) * bfhi(gwv[1][e]));
        }
#pragma unroll
        for (int o = 32; o >= 1; o >>= 1) s += __shfl_xor(s, o);
        sg2[r] = s;
      }
      const float s = rbf((lane & 1) ? sg2[1] : sg2[0]);
      const float g = rbf(1.0f / (1.0f + __expf(-s)));
      __builtin_amdgcn_raw_buffer_store_b32(__float_as_uint(g), rws, WS_G + (b * 8 + wid * 2 + (lane & 1)) * 4, 0, SC1);
    }
    WAIT_V(0);
    flag_set(ws, WS_FLAG + ((lane & 16) ? (unsigned)((b >> 4) * 16 + (lane & 15)) * FL_STRIDE + FL_G + ((b & 15) * 4 + wid) * 8
                                        : (unsigned)((lane & 15) * 16 + (ch >> 6)) * FL_STRIDE + FL_Z + (ch & 63) * 8), tag);
    STAMP_AT(5);
    *(volatile LAS unsigned*)(lds + L_CTRL + 32 + wid * 4) = 1u;
    LAS char* sg = lds + wid * CV_SIZE + L_STG;
    LAS char* zs = lds + wid * CV_SIZE + L_ZS;
#pragma unroll
    for (int i = 0; i < 4; ++i) *(LAS u32x4*)(sg + (i * 8 + (lane >> 3)) * 144 + (lane & 7) * 16) = qr[i];
    WAIT_L(0);
    u32x2 qv[2][4];
#pragma unroll
    for (int i2 = 0; i2 < 2; ++i2)
#pragma unroll
      for (int n = 0; n < 4; ++n) qv[i2][n] = *(LAS u32x2*)(sg + (i2 * 16 + fr) * 144 + n * 32 + fq * 8);
    lds_wait4<16>(lds + L_CTRL + 48);
    {
      const unsigned tl = (unsigned)tag, th = (unsigned)(tag >> 32);
      for (unsigned spins = 0; spins < (1u << 16); ++spins) {
        const u32x4 v = ld16(rws, lane * 16, myfl + FL_G);
        if (__all(v[0] == tl && v[1] == th && v[2] == tl && v[3] == th)) break;
        __builtin_amdgcn_s_sleep(16);
      }
      asm volatile("" ::: "memory");
      __builtin_amdgcn_fence(__ATOMIC_ACQUIRE, "wavefront");
    }
    float gv[2];
    {
      u32x4 zr[4];
#pragma unroll
      for (int i2 = 0; i2 < 2; ++i2) gv[i2] = __uint_as_float(__builtin_amdgcn_raw_buffer_load_b32(rws, (rt * 128 + (wid * 2 + i2) * 16 + fr) * 4, WS_G, SC1));
#pragma unroll
      for (int i = 0; i < 4; ++i) zr[i] = ld16(rws, (unsigned)(ct * 65 + i * 16 + (lane >> 2)) * 4096u + (lane & 3) * 16, WS_ZT + (unsigned)(rt * 128 + wid * 32) * 2u);
#pragma unroll
      for (int i = 0; i < 4; ++i) *(LAS u32x4*)(zs + (i * 16 + (lane >> 2)) * 80 + (lane & 3) * 16) = zr[i];
    }
    WAIT_L(0);
    const int q4 = (lane & 15) >> 2, p = lane & 3;
#pragma unroll
    for (int i2 = 0; i2 < 2; ++i2) {
      const float g = gv[i2];
#pragma unroll
      for (int n = 0; n < 4; ++n) {
        const s16x4 zz = __builtin_amdgcn_ds_read_tr16_b64_v4i16((LAS s16x4*)(zs + (16 * n + 4 * fq + q4) * 80 + (i2 * 16 + 4 * p) * 2));
        const u32x2 zu = __builtin_bit_cast(u32x2, zz);
        const unsigned q01 = qv[i2][n][0], q23 = qv[i2][n][1];
        f32x4 ga;
        ga[0] = rbf(g * rbf(bflo(q01) * bflo(zu[0]))); ga[1] = rbf(g * rbf(bfhi(q01) * bfhi(zu[0])));
        ga[2] = rbf(g * rbf(bflo(q23) * bflo(zu[1]))); ga[3] = rbf(g * rbf(bfhi(q23) * bfhi(zu[1])));
        *(LAS f32x4*)(sg + (i2 * 4 + n) * 1024 + lane * 16) = ga;
      }
      *(LAS float*)(sg + 8192 + i2 * 256 + lane * 4) = rbf(1.0f - g);
    }
    WAIT_L(0);
    STAMP_AT(6);
    BAR();
    if (wid < 3) *(u32x4*)(ws + myfl + (wid * 64 + lane) * 16) = (u32x4){0u, 0u, 0u, 0u};
    STAMP_AT(7);
  }
}

extern "C" void kernel_launch(void* const* d_in, const int* in_sizes, int n_in, void* d_out, int out_size, void* d_ws, size_t ws_size,
                              hipStream_t stream) {
  const u16* q = (const u16*)d_in[0]; const u16* kk = (const u16*)d_in[1]; const u16* vv = (const u16*)d_in[2];
  const u16* shk = (const u16*)d_in[3]; const u16* ssmk = (const u16*)d_in[4]; const u16* ssmD = (const u16*)d_in[5];
  const u16* wg = (const u16*)d_in[7]; const u16* wu = (const u16*)d_in[8]; const u16* wd = (const u16*)d_in[9];
  const u16* gw = (const u16*)d_in[10];
  constexpr int LDS1 = 163840;
  (void)hipFuncSetAttribute((const void*)k1_kernel, hipFuncAttributeMaxDynamicSharedMemorySize, LDS1);
  k1_kernel<<<256, 512, LDS1, stream>>>(q, kk, vv, shk, ssmk, ssmD, wg, wu, wd, gw, (char*)d_ws, (u16*)d_out);
}
```

```cpp
#define G2_N0 7
#define WARM 1
#include <hip/hip_runtime.h>
#include <hip/hip_bf16.h>

typedef unsigned short u16;
typedef unsigned long long u64;
typedef short s16x4 __attribute__((ext_vector_type(4)));
typedef short s16x8 __attribute__((ext_vector_type(8)));
typedef __bf16 bfx2 __attribute__((ext_vector_type(2)));
typedef float f32x4 __attribute__((ext_vector_type(4)));
typedef unsigned u32x4 __attribute__((ext_vector_type(4)));
typedef unsigned u32x2 __attribute__((ext_vector_type(2)));
typedef __amdgpu_buffer_rsrc_t rsrc_t;
#define LAS __attribute__((address_space(3)))
#define GAS __attribute__((address_space(1)))
#define DEV __device__ __forceinline__
#define AI __attribute__((always_inline))
extern "C" __device__ u64 hw_dispatch_id() __asm("llvm.amdgcn.dispatch.id");

constexpr int S_ = 2048, D_ = 1024, F_ = 3584;
constexpr unsigned WS_HF = 0;
constexpr unsigned HF_KS = 129 * 1024;
constexpr unsigned WS_KVT = 16u << 20;
constexpr unsigned WS_ZT = 21u << 20;
constexpr unsigned WS_G = 26u << 20;
constexpr unsigned WS_FLAG = 27u << 20;
constexpr unsigned WS_BYTES = 28u << 20;
__device__ constexpr unsigned ch_off(unsigned ch) { return (ch + (ch >> 6)) * 4096u; }
constexpr int FL_H = 0, FL_G = 1024, FL_Z = 1536, FL_KV = 2048, FL_BYTES = 2560;
constexpr unsigned FL_STRIDE = 4096;
constexpr int SC1 = 16;
constexpr int L_PFS = 163840 - 512, L_CTRL = 163840 - 64;

DEV float bflo(unsigned w) { return __uint_as_float(w << 16); }
DEV float bfhi(unsigned w) { return __uint_as_float(w & 0xffff0000u); }
DEV unsigned pack2(float lo, float hi) { bfx2 p; p[0] = (__bf16)lo; p[1] = (__bf16)hi; return __builtin_bit_cast(unsigned, p); }
DEV float rbf(float f) { return (float)(__bf16)f; }
DEV void mfma16a(f32x4& c, s16x8 a, s16x8 b) {
  asm volatile("v_mfma_f32_16x16x32_bf16 %0, %1, %2, %0" : "+v"(c) : "v"(a), "v"(b));
}
DEV void mfma16z(f32x4& c, s16x8 a, s16x8 b) {
  asm volatile("v_mfma_f32_16x16x32_bf16 %0, %1, %2, 0" : "=&v"(c) : "v"(a), "v"(b));
}
DEV s16x8 tr_read8(LAS char* p0, LAS char* p1) {
  s16x4 lo = __builtin_amdgcn_ds_read_tr16_b64_v4i16((LAS s16x4*)p0);
  s16x4 hi = __builtin_amdgcn_ds_read_tr16_b64_v4i16((LAS s16x4*)p1);
  return __builtin_shufflevector(lo, hi, 0, 1, 2, 3, 4, 5, 6, 7);
}
template <int OFF, int D2>
DEV s16x8 tr_read8_asm(unsigned addr) {
  s16x4 lo, hi;
  asm volatile("ds_read_b64_tr_b16 %0, %2 offset:%3\n\tds_read_b64_tr_b16 %1, %2 offset:%4" : "=&v"(lo), "=&v"(hi) : "v"(addr), "i"(OFF), "i"(OFF + D2));
  return __builtin_shufflevector(lo, hi, 0, 1, 2, 3, 4, 5, 6, 7);
}
template <int OFF>
DEV s16x8 ds_read16_asm(unsigned addr) {
  s16x8 r;
  asm volatile("ds_read_b128 %0, %1 offset:%2" : "=v"(r) : "v"(addr), "i"(OFF));
  return r;
}
template <int V> struct IC { static constexpr int value = V; };
DEV rsrc_t mk_rsrc(const void* p, unsigned bytes) { return __builtin_amdgcn_make_buffer_rsrc((void*)p, 0, bytes, 0x00020000); }
DEV void blds16(rsrc_t r, LAS char* l, unsigned voff, unsigned soff) {
  __builtin_amdgcn_raw_ptr_buffer_load_lds(r, (LAS void*)l, 16, voff, soff, 0, 0);
}
DEV void blds4(rsrc_t r, LAS char* l, unsigned voff, unsigned soff) {
  __builtin_amdgcn_raw_ptr_buffer_load_lds(r, (LAS void*)l, 4, voff, soff, 0, 0);
}
DEV u32x4 ld16(rsrc_t r, unsigned voff, unsigned soff) { return __builtin_amdgcn_raw_buffer_load_b128(r, voff, soff, SC1); }
DEV void st16(rsrc_t r, unsigned voff, u32x4 v) { __builtin_amdgcn_raw_buffer_store_b128(v, r, voff, 0, SC1); }
#define STAMP_AT(i)
#define WAIT_V(n) asm volatile("s_waitcnt vmcnt(" #n ")" ::: "memory")
#define WAIT_L(n) asm volatile("s_waitcnt lgkmcnt(" #n ")" ::: "memory")
#define BAR() __builtin_amdgcn_s_barrier()
#define SCHED() __builtin_amdgcn_sched_barrier(0)
#define MFMA_DRAIN() do { asm volatile("s_nop 7\n\ts_nop 7" ::: "memory"); SCHED(); } while (0)

DEV void flag_set(char* ws, unsigned off, u64 tag) { __hip_atomic_store((GAS u64*)(ws + off), tag, __ATOMIC_RELAXED, __HIP_MEMORY_SCOPE_AGENT); }
DEV u64 flag_peek(char* ws, unsigned off, int n) {
  const int lane = threadIdx.x & 63;
  return __hip_atomic_load((GAS u64*)(ws + off + 8 * (lane < n ? lane : 0)), __ATOMIC_RELAXED, __HIP_MEMORY_SCOPE_AGENT);
}
template <int SLP>
DEV void flag_wait(u64 first, char* ws, unsigned off, int n, u64 tag) {
  u64 v = first;
  for (unsigned spins = 0; spins < (1u << 16); ++spins) {
    if (__all(v == tag)) break;
    __builtin_amdgcn_s_sleep(SLP);
    v = flag_peek(ws, off, n);
  }
  __builtin_amdgcn_fence(__ATOMIC_ACQUIRE, "wavefront");
}
template <int SLP = 1>
DEV void lds_wait4(LAS char* p) {
  for (unsigned spins = 0; spins < (1u << 22); ++spins) {
    const u32x4 dn = *(volatile LAS u32x4*)p;
    if (__builtin_amdgcn_readfirstlane(dn[0] & dn[1] & dn[2] & dn[3]) != 0u) break;
    __builtin_amdgcn_s_sleep(SLP);
  }
  asm volatile("" ::: "memory");
}
DEV int tile_of_block(int b) { const int xcd = b & 7, j = b >> 3; return ((xcd >> 1) * 4 + (j >> 3)) * 16 + (xcd & 1) * 8 + (j & 7); }

struct Args {
  const u16 *q, *kk, *vv, *shk, *ssmk, *ssmD, *wg, *wu, *wd, *gw;
  char* ws; u16* out;
};

DEV void gemm1_main(f32x4 (&acc)[4][8], u64& kvpeek, unsigned kvoff, LAS char* lds, const Args& a, int pm, int pn, int im, int in_) {
  char* ws = a.ws;
  const int tid = threadIdx.x, lane = tid & 63;
  const int wid = __builtin_amdgcn_readfirstlane(tid >> 6);
  const int wr = wid >> 1, wc = wid & 1;
  const int fr = lane & 15, fq = lane >> 4;
  unsigned va[2], vb[2];
#pragma unroll
  for (int e = 0; e < 2; ++e) {
    const int rl = lane >> 3, p = lane & 7;
    const int sw = (e << 2) | (lane >> 4);
    va[e] = (unsigned)(rl * 2048 + ((p ^ sw) * 16));
    const int kl = lane >> 4, pb = lane & 15;
    const int swb = (kl | (e << 2)) << 1;
    vb[e] = (unsigned)(kl * (F_ * 2) + (pb ^ swb) * 16);
  }
  const rsrc_t rx = mk_rsrc(a.q, S_ * D_ * 2);
  const rsrc_t rw = mk_rsrc((wid < 4) ? a.wg : a.wu, D_ * F_ * 2);
  const unsigned xa_base = (unsigned)(pm * 256 + wid * 32) * 2048u;
  const unsigned wb_base = (unsigned)((wid & 3) * 16) * (F_ * 2) + pn * 256;
  LAS char* ldsA = lds + wid * 4096;
  LAS char* ldsB = lds + 65536 + wid * 4096;
  auto dma = [&](int kt, int st, int d) AI {
    const int i = d >> 1;
    if (d & 1) blds16(rw, ldsB + st * 32768 + i * 1024, vb[i >> 1], wb_base + (kt * 64 + i * 4) * (F_ * 2));
    else blds16(rx, ldsA + st * 32768 + i * 1024, va[i & 1], xa_base + i * (8 * 2048) + kt * 128);
  };
#pragma unroll
  for (int d = 0; d < 8; ++d) dma(0, 0, d);
#pragma unroll
  for (int d = 0; d < 8; ++d) dma(1, 1, d);
  SCHED();
  rsrc_t rp; unsigned vp, pstep;
  if (wid < 4) {
    rp = mk_rsrc((im >> 1) ? a.wu : a.wg, D_ * F_ * 2);
    vp = (unsigned)(wid * 16 + (lane & 15)) * (F_ * 2) + pn * 256 + (im & 1) * 128; pstep = 64 * F_ * 2;
  } else {
    rp = rx;
    int r = in_ * 37 + (wid - 4) * 10 + ((lane & 15) < 10 ? (lane & 15) : 9); r = r > 255 ? 255 : r;
    vp = (unsigned)(pm * 256 + r) * 2048u; pstep = 128;
  }
  vp += (unsigned)(lane >> 4) * 32u;
  auto pf = [&](int kt) AI { blds4(rp, lds + L_PFS, vp, kt * pstep); };
  const unsigned lds0 = (unsigned)(size_t)lds;
  unsigned bx[2];
#pragma unroll
  for (int ks = 0; ks < 2; ++ks) bx[ks] = lds0 + (wr * 64 + fr) * 128 + (((ks * 4 + fq) ^ (fr >> 1)) * 16);
  unsigned bw[4];
  {
    const int q = (lane & 15) >> 2, p = lane & 3;
    const int sw = (q | ((fq & 1) << 2)) << 1;
#pragma unroll
    for (int j = 0; j < 4; ++j) bw[j] = lds0 + 65536 + (8 * fq + q) * 256 + (((wc * 8 + j * 2 + (p >> 1)) ^ sw) * 16) + (p & 1) * 8;
  }
  auto ldx = [&](auto ST, auto KS, const int i) AI -> s16x8 {
    constexpr int o = decltype(ST)::value * 32768; const unsigned p = bx[decltype(KS)::value];
    return i == 0 ? ds_read16_asm<o>(p) : i == 1 ? ds_read16_asm<o + 2048>(p) : i == 2 ? ds_read16_asm<o + 4096>(p) : ds_read16_asm<o + 6144>(p);
  };
  auto ldw = [&](auto ST, auto KS, const int n) AI -> s16x8 {
    constexpr int o = decltype(ST)::value * 32768 + decltype(KS)::value * 8192;
    return n < 4 ? tr_read8_asm<o, 1024>(bw[n & 3]) : tr_read8_asm<o + 16384, 1024>(bw[n & 3]);
  };
  s16x8 xa[4], xb[4], w[8];
  WAIT_V(8); BAR(); SCHED();
  STAMP_AT(1);
#pragma unroll
  for (int i = 0; i < 4; ++i) xa[i] = ldx(IC<0>{}, IC<0>{}, i);
#pragma unroll
  for (int n = 0; n < 8; ++n) w[n] = ldw(IC<0>{}, IC<0>{}, n);
  pf(2); pf(3); pf(4);
  WAIT_L(0);
  SCHED();

  auto tile_body = [&](auto ST, const int kt, const bool pre1, const bool pre2, const int hook) AI {
    constexpr int st = decltype(ST)::value;
#pragma unroll
    for (int n = 0; n < 8; ++n) {
      if (pre2) { if (n == 2) __builtin_amdgcn_s_setprio(1); if (n == 6) __builtin_amdgcn_s_setprio(0); }
      if (n == 6) WAIT_L(15);
#pragma unroll
      for (int i = 0; i < 4; ++i) {
        if (n == 0) { if (i == 0) WAIT_L(13); if (i == 1) WAIT_L(10); if (i == 2) WAIT_L(7); if (i == 3) WAIT_L(4); }
        if (hook == 1) mfma16z(acc[i][n], w[n], xa[i]); else mfma16a(acc[i][n], w[n], xa[i]);
      }
      w[n] = ldw(ST, IC<1>{}, n);
      if (n < 4) xb[n] = ldx(ST, IC<1>{}, n);
      if (n == 3 && hook == 0) pf(kt + 4);
      if (n == 3 && hook == 5) kvpeek = flag_peek(ws, kvoff, 32);
      SCHED();
    }
#pragma unroll
    for (int n = 0; n < 2; ++n) {
#pragma unroll
      for (int i = 0; i < 4; ++i) {
        if (n == 0) { if (i == 0) WAIT_L(15); if (i == 1) WAIT_L(14); if (i == 2) WAIT_L(11); if (i == 3) WAIT_L(8); }
        mfma16a(acc[i][n], w[n], xb[i]);
      }
    }
    SCHED();
    WAIT_L(0);
    if (pre1) {
      if (hook == 1) { WAIT_V(3); } else if (hook == 7) { WAIT_V(0); } else { WAIT_V(1); }
      BAR(); SCHED();
      if (pre2) __builtin_amdgcn_s_setprio(3);
      else if (wid < 4) __builtin_amdgcn_s_setprio(2);
      else __builtin_amdgcn_s_setprio(1);
      w[0] = ldw(IC<(st ^ 1)>{}, IC<0>{}, 0); w[1] = ldw(IC<(st ^ 1)>{}, IC<0>{}, 1);
      if (pre2) { dma(kt + 2, st, 0); dma(kt + 2, st, 1); }
      SCHED();
    }
#pragma unroll
    for (int n = 2; n < 8; ++n) {
      if (pre2 && n == 6) __builtin_amdgcn_s_setprio(2);
#pragma unroll
      for (int i = 0; i < 4; ++i) mfma16a(acc[i][n], w[n], xb[i]);
      if (pre1) {
        w[n] = ldw(IC<(st ^ 1)>{}, IC<0>{}, n);
        if (n - 2 < 4) xa[n - 2] = ldx(IC<(st ^ 1)>{}, IC<0>{}, n - 2);
        if (pre2) dma(kt + 2, st, n);
      }
      SCHED();
    }
  };
  tile_body(IC<0>{}, 0, true, true, 1);
  tile_body(IC<1>{}, 1, true, true, 0);
  for (int kt = 2; kt < 12; kt += 2) {
    tile_body(IC<0>{}, kt, true, true, 0);
    tile_body(IC<1>{}, kt + 1, true, true, 0);
  }
  tile_body(IC<0>{}, 12, true, true, 5);
  tile_body(IC<1>{}, 13, true, true, 7);
  tile_body(IC<0>{}, 14, true, false, 7);
  tile_body(IC<1>{}, 15, false, false, 7);
  *(volatile LAS unsigned*)(lds + L_CTRL + wid * 4) = 1u;
  MFMA_DRAIN();
  STAMP_AT(2);
}

constexpr unsigned CODE_BYTES = 56 * 1024;
DEV void light_unit(LAS char* lds, const Args& a, rsrc_t rws, u64 tag, int cu, const void* codebase) {
  char* ws = a.ws;
  const int tid = threadIdx.x, lane = tid & 63;
  const int wid = __builtin_amdgcn_readfirstlane(tid >> 6);
  const int cb = cu & 7, tblk = (cu >> 3) * 8 + wid;
  const int kch = cb * 128 + lane * 2;
  LAS char* wl = lds + wid * (128 * 144);
  if (wid == 7) {
    const rsrc_t rc = mk_rsrc(codebase, CODE_BYTES);
#pragma unroll
    for (int i = 0; i < 2; ++i) blds4(rc, lds + L_PFS, lane * 128, ((cu & 3) * 2 + i) * 8192);
    if ((cu & 3) == 0) {
      blds4(rws, lds + L_PFS, (lane < 56 ? lane : 55) * (2 * HF_KS) + 131072, WS_HF);
      blds4(rws, lds + L_PFS, (lane & 15) * (65 * 4096u) + 64 * 4096u + ((lane & 16) ? WS_ZT : WS_KVT), 0);
      blds4(mk_rsrc(a.out, S_ * D_ * 2), lds + L_PFS, lane * 65536, 0);
    }
  }
  const u32x4 shw = *(const u32x4*)(a.shk + kch * 4);
#pragma unroll
  for (int hf = 0; hf < 2; ++hf) {
    const int t0 = tblk * 64 + hf * 32;
    unsigned kr[35], vr[32];
#pragma unroll
    for (int jx = 0; jx < 35; ++jx) { const int t = t0 - 3 + jx; kr[jx] = *(const unsigned*)(a.kk + (size_t)(t < 0 ? 0 : t) * D_ + kch); }
#pragma unroll
    for (int jx = 0; jx < 32; ++jx) vr[jx] = *(const unsigned*)(a.vv + (size_t)(t0 + jx) * D_ + kch);
    SCHED();
#pragma unroll
    for (int c = 0; c < 2; ++c) {
      const unsigned s01 = shw[c * 2], s23 = shw[c * 2 + 1];
      const float w0 = bflo(s01), w1 = bfhi(s01), w2 = bflo(s23), w3 = bfhi(s23);
#pragma unroll
      for (int sb = 0; sb < 4; ++sb) {
        float kf[11];
#pragma unroll
        for (int jx = 0; jx < 11; ++jx) { kf[jx] = c ? bfhi(kr[sb * 8 + jx]) : bflo(kr[sb * 8 + jx]); if (sb == 0 && jx < 3 && t0 == 0) kf[jx] = 0.f; }
        float o[8];
#pragma unroll
        for (int jx = 0; jx < 8; ++jx) o[jx] = (kf[jx] * w3 + kf[jx + 1] * w2 + kf[jx + 2] * w1 + kf[jx + 3] * w0) * (c ? bfhi(vr[sb * 8 + jx]) : bflo(vr[sb * 8 + jx]));
        *(LAS u32x4*)(wl + (lane * 2 + c) * 144 + (hf * 4 + sb) * 16) = (u32x4){pack2(o[7], o[6]), pack2(o[5], o[4]), pack2(o[3], o[2]), pack2(o[1], o[0])};
      }
    }
  }
  WAIT_L(0);
#pragma unroll
  for (int e = 0; e < 16; ++e) {
    const int c = e * 8 + (lane >> 3);
    const u32x4 v = *(LAS u32x4*)(wl + c * 144 + (lane & 7) * 16);
    st16(rws, WS_KVT + ch_off(cb * 128 + c) + tblk * 128 + (lane & 7) * 16, v);
  }
  WAIT_V(0);
  flag_set(ws, WS_FLAG + (unsigned)tile_of_block(cb * 32 + (lane & 31)) * FL_STRIDE + FL_KV + tblk * 8, tag);
  STAMP_AT(1);
  const rsrc_t rd = mk_rsrc(a.wd, F_ * D_ * 2), rk = mk_rsrc(a.ssmk, D_ * S_ * 2);
#pragma unroll
  for (int i = 0; i < 2; ++i) blds4(rk, lds + L_PFS, tid * 128, cu * 131072 + i * 65536);
#pragma unroll
  for (int i = 0; i < 4; ++i) blds4(rd, lds + L_PFS, tid * 128, (i * 32 + cu) * 57344);
  WAIT_V(0);
  STAMP_AT(2);
  __syncthreads();
}

DEV u32x2 geglu4(f32x4 gt, f32x4 up) {
  const float C1 = -2.302208198f, C2 = -2.302208198f * 0.044715f;
  const f32x4 wv = gt * (gt * gt * C2 + C1);
  f32x4 ev;
#pragma unroll
  for (int r = 0; r < 4; ++r) ev[r] = __builtin_amdgcn_exp2f(wv[r]);
  const f32x4 dv = ev + 1.0f;
  f32x4 rv;
#pragma unroll
  for (int r = 0; r < 4; ++r) rv[r] = __builtin_amdgcn_rcpf(dv[r]);
  const f32x4 hv = gt * up * rv;
  return (u32x2){pack2(hv[0], hv[1]), pack2(hv[2], hv[3])};
}
DEV void gemm1_epi(const f32x4 (&acc)[4][8], rsrc_t rws, int pm, int pn) {
  const int lane = threadIdx.x & 63;
  const int wid = __builtin_amdgcn_readfirstlane(threadIdx.x >> 6);
  const int wr = wid >> 1, wc = wid & 1;
  const unsigned off = WS_HF + (unsigned)(pn * 4 + wc * 2) * HF_KS + (unsigned)(pm * 16 + wr * 4) * 1024u + lane * 16;
#pragma unroll
  for (int i = 0; i < 4; ++i)
#pragma unroll
    for (int k2 = 0; k2 < 2; ++k2) {
      const u32x2 h0 = geglu4(acc[i][k2 * 2], acc[i][4 + k2 * 2]), h1 = geglu4(acc[i][k2 * 2 + 1], acc[i][4 + k2 * 2 + 1]);
      st16(rws, off + k2 * HF_KS + i * 1024, (u32x4){h0[0], h0[1], h1[0], h1[1]});
    }
}

constexpr int CV_CS = 4160, CV_U = 4 * CV_CS  , CV_SIZE = 23552;
struct ConvIn { u32x4 own[4]; u32x2 nxt[4], h0; u32x4 u[4]; };

DEV void conv_write_seg(LAS char* wl, int seg, u32x4 a, u32x2 b) {
  const unsigned a01 = __builtin_amdgcn_alignbit(a[1], a[0], 16), a12 = __builtin_amdgcn_alignbit(a[2], a[1], 16),
                 a23 = __builtin_amdgcn_alignbit(a[3], a[2], 16), a34 = __builtin_amdgcn_alignbit(b[0], a[3], 16),
                 a45 = __builtin_amdgcn_alignbit(b[1], b[0], 16);
  LAS char* dst = wl + seg * 16;
  *(LAS u32x4*)(dst) = a;
  *(LAS u32x4*)(dst + CV_CS) = (u32x4){a01, a12, a23, a34};
  *(LAS u32x4*)(dst + 2 * CV_CS) = (u32x4){a[1], a[2], a[3], b[0]};
  *(LAS u32x4*)(dst + 3 * CV_CS) = (u32x4){a12, a23, a34, a45};
}
DEV s16x8 conv_ldp(LAS char* p) {
  volatile LAS s16x4* pp = (volatile LAS s16x4*)p;
  const s16x4 lo = pp[0], hi = pp[1];
  return __builtin_shufflevector(lo, hi, 0, 1, 2, 3, 4, 5, 6, 7);
}

DEV void conv_wave(LAS char* wl, const ConvIn& in, float dv, rsrc_t rws, int ch) {
  const int lane = threadIdx.x & 63;
  const int fr = lane & 15, fq = lane >> 4;
  const u32x4 zero4 = (u32x4){0u, 0u, 0u, 0u};
#pragma unroll
  for (int p = 0; p < 4; ++p) conv_write_seg(wl, 64 * p + lane + 2, in.own[p], in.nxt[p]);
  conv_write_seg(wl, lane & 1, zero4, (lane & 1) ? in.h0 : (u32x2){0u, 0u});
  *(LAS u32x4*)(wl + CV_U + (lane & 31) * 16) = zero4;
#pragma unroll
  for (int i = 0; i < 4; ++i) *(LAS u32x4*)(wl + CV_U + 512 + i * 1024 + lane * 16) = in.u[i];
  const int o_ = 1 + fr + 8 * fq;
  LAS char* pb = wl + (o_ & 3) * CV_CS + 2 * (o_ & ~3);
  LAS char* qb = wl + CV_U + 16 * (33 + 2 * fr - fq);
  f32x4 acc[8];
#pragma unroll
  for (int g = 0; g < 8; ++g) acc[g] = (f32x4){0.f, 0.f, 0.f, 0.f};
  s16x8 P0[8], Q0[8], P1[8], Q1[8];
  WAIT_L(0); SCHED();
#pragma unroll
  for (int a = 0; a < 8; ++a) { P0[a] = conv_ldp(pb + a * 512); Q0[a] = *(LAS s16x8*)(qb + a * 512); }
  SCHED();
  auto cls = [&](s16x8 (&P)[8], s16x8 (&Q)[8], s16x8 (&Pn)[8], s16x8 (&Qn)[8], LAS char* pn, LAS char* qn) AI {
#pragma unroll
    for (int b = 0; b < 8; ++b) {
      Pn[b] = conv_ldp(pn + b * 512); Qn[b] = *(LAS s16x8*)(qn + b * 512);
#pragma unroll
      for (int a = 0; a + b < 8; ++a) mfma16a(acc[a + b], P[a], Q[b]);
      SCHED();
    }
  };
  for (int r = 0; r < 8; r += 2) {
    cls(P0, Q0, P1, Q1, pb + (r + 1) * 64, qb - (r + 1) * 64);
    const int rn = (r + 2) & 7;
    cls(P1, Q1, P0, Q0, pb + rn * 64, qb - rn * 64);
  }
  MFMA_DRAIN();
#pragma unroll
  for (int e = 0; e < 4; ++e) {
    unsigned zx[2][2];
#pragma unroll
    for (int h = 0; h < 2; ++h) {
      const int g = 2 * e + h;
      const int al = 32 * g + 2 * fr + (fq >> 1);
      const u32x2 kvp = *(LAS u32x2*)(wl + CV_U + 512 + al * 16 + ((fq & 1) ? 0 : 8));
      const float k3 = bflo(kvp[0]), k2 = bfhi(kvp[0]), k1 = bflo(kvp[1]), k0 = bfhi(kvp[1]);
      const float z0 = rbf(acc[g][0]) + rbf(k0 * dv), z1 = rbf(acc[g][1]) + rbf(k1 * dv), z2 = rbf(acc[g][2]) + rbf(k2 * dv), z3 = rbf(acc[g][3]) + rbf(k3 * dv);
      zx[h][0] = pack2(z0, z1); zx[h][1] = pack2(z2, z3);
    }
    const auto r0 = __builtin_amdgcn_permlane16_swap(zx[0][0], zx[1][0], false, false);
    const auto r1 = __builtin_amdgcn_permlane16_swap(zx[0][1], zx[1][1], false, false);
    st16(rws, WS_ZT + ch_off(ch) + (unsigned)(256 * (2 * e + (fq & 1)) + 16 * fr + 8 * (fq >> 1)) * 2u, (u32x4){r0[0], r1[0], r0[1], r1[1]});
  }
}

constexpr int L_STG = 8192, L_ZS = L_STG + 9216, L_RING = 4 * CV_SIZE;
static_assert(L_ZS + 5120 <= CV_SIZE && CV_U + 4608 <= CV_SIZE && L_RING + 65536 <= L_PFS, "lds map");

__global__ __launch_bounds__(512, 2) void k1_kernel(const u16* __restrict__ q, const u16* __restrict__ kk, const u16* __restrict__ vv,
                                                    const u16* __restrict__ shk, const u16* __restrict__ ssmk, const u16* __restrict__ ssmD,
                                                    const u16* __restrict__ wg, const u16* __restrict__ wu, const u16* __restrict__ wd,
                                                    const u16* __restrict__ gw, char* ws, u16* __restrict__ out) {
  extern __shared__ __attribute__((aligned(16))) char shm[];
  LAS char* lds = (LAS char*)shm;
  u64 pc0; asm volatile("s_getpc_b64 %0" : "=s"(pc0));
  const void* codebase = (const void*)(pc0 & ~255ull);
  const int tid = threadIdx.x, lane = tid & 63;
  const int wid = __builtin_amdgcn_readfirstlane(tid >> 6);
  const int fr = lane & 15, fq = lane >> 4;
  const int b = blockIdx.x, xcd = b & 7, j = b >> 3;
  const u64 tag = ((hw_dispatch_id() * 0x9E3779B97F4A7C15ull) ^ 0xD1B54A32D192ED03ull) | 1ull;
  const rsrc_t rws = mk_rsrc(ws, WS_BYTES);
  const int rt = (xcd >> 1) * 4 + (j >> 3), ct = (xcd & 1) * 8 + (j & 7);
  const unsigned myfl = WS_FLAG + (unsigned)(rt * 16 + ct) * FL_STRIDE;
  const int ch = b * 4 + (wid & 3);
  const bool light = j < 4;
  const int jj = light ? 0 : j - 4;
  const int pm = (xcd & 1) * 4 + (jj & 3), pn = (xcd >> 1) * 7 + (jj >> 2);
  const Args a{q, kk, vv, shk, ssmk, ssmD, wg, wu, wd, gw, ws, out};
  STAMP_AT(0);

  if (tid < 16) *(volatile LAS unsigned*)(lds + L_CTRL + tid * 4) = (light && tid < 8) ? 1u : 0u;
  ConvIn in;
  const u16* hrow = ssmk + (size_t)ch * S_;
  unsigned dvraw = 0;
  auto conv_issue_h = [&]() AI {
#pragma unroll
    for (int p = 0; p < 4; ++p) {
      in.own[p] = *(const u32x4*)(hrow + 8 * (64 * p + lane));
      const int nx = 64 * p + lane + 1;
      in.nxt[p] = *(const u32x2*)(hrow + 8 * (nx < 256 ? nx : 255));
    }
    in.h0 = *(const u32x2*)hrow;
    dvraw = ssmD[ch];
  };
  auto conv_issue_u = [&]() AI {
#pragma unroll
    for (int i = 0; i < 4; ++i) in.u[i] = ld16(rws, lane * 16 + i * 1024, WS_KVT + ch_off(ch));
  };
  u64 kv0 = 0;
  if (light) {
    light_unit(lds, a, rws, tag, xcd * 4 + j, codebase);
    conv_issue_h();
    flag_wait<4>(kv0, ws, myfl + FL_KV, 32, tag);
    conv_issue_u();
  } else {
    f32x4 acc1[4][8];
    gemm1_main(acc1, kv0, myfl + FL_KV, lds, a, pm, pn, jj & 3, jj >> 2);
    flag_wait<4>(kv0, ws, myfl + FL_KV, 32, tag);
    conv_issue_h();
    conv_issue_u();
    SCHED();
    gemm1_epi(acc1, rws, pm, pn);
    if (wid < 4) __builtin_amdgcn_s_setprio(0);
    WAIT_V(0);
    flag_set(ws, WS_FLAG + (unsigned)((pm * 2 + (wid >> 2)) * 16 + (lane & 15)) * FL_STRIDE + FL_H + (((wid & 1) * 28 + pn) * 2 + ((wid >> 1) & 1)) * 8, tag);
  }
  STAMP_AT(3);
  lds_wait4(lds + L_CTRL); lds_wait4(lds + L_CTRL + 16);

  if (wid >= 4) {
    const int w4 = wid - 4;
    const unsigned lo16 = lane * 16;
    const unsigned ha = WS_HF + (unsigned)w4 * HF_KS + (unsigned)(rt * 8) * 1024u;
    const rsrc_t rd = mk_rsrc(wd, F_ * D_ * 2);
    unsigned vbs[2];
#pragma unroll
    for (int gp = 0; gp < 2; ++gp)
      vbs[gp] = (unsigned)((lane >> 3) & 3) * 2048u + (((lane & 7) ^ ((((lane >> 4) & 1) | (gp << 1)) << 1)) * 16) + (lane >> 5) * (16 * 2048u);
    LAS char* ring = lds + L_RING + w4 * 16384;
    unsigned bb[4];
    {
      const int q4 = (lane & 15) >> 2, p = lane & 3;
      const int f2 = (q4 >> 1) | ((fq & 1) << 1);
#pragma unroll
      for (int n = 0; n < 4; ++n) bb[n] = (unsigned)(size_t)ring + (8 * fq + q4) * 128 + ((((n ^ f2) << 1) + (p >> 1)) * 16) + (p & 1) * 8;
    }
    s16x8 A[2][8], B[2][4];
    auto ldA = [&](int s, int i) AI { return __builtin_bit_cast(s16x8, ld16(rws, lo16 + (i & 3) * 1024, ha + (unsigned)s * (4 * HF_KS) + (i >> 2) * 4096)); };
    auto dmaB = [&](int s, int slot) AI {
#pragma unroll
      for (int g = 0; g < 4; ++g) blds16(rd, ring + slot * 4096 + g * 1024, vbs[g & 1], (unsigned)((4 * s + w4) * 32 + 4 * g) * 2048u + ct * 128);
    };
    auto ldB = [&](const int slot, int n) AI {
      return slot == 0 ? tr_read8_asm<0, 512>(bb[n]) : slot == 1 ? tr_read8_asm<4096, 512>(bb[n]) : slot == 2 ? tr_read8_asm<8192, 512>(bb[n]) : tr_read8_asm<12288, 512>(bb[n]);
    };
    const unsigned hfo = myfl + FL_H + (w4 >> 1) * 448;
    u64 rdy = 0; int nxt = 0;
    auto poll_eval = [&](u64 v) AI {
      rdy = __ballot(v == tag);
      __builtin_amdgcn_fence(__ATOMIC_ACQUIRE, "wavefront");
    };
    auto need = [&](const int upto) AI {
      const u64 mask = (1ull << (2 * upto)) - 1ull;
      for (unsigned spins = 0; (rdy & mask) != mask && spins < (1u << 16); ++spins) {
        if (spins) __builtin_amdgcn_s_sleep(4);
        poll_eval(flag_peek(ws, hfo, 56));
      }
    };
    auto next = [&]() AI -> int { return nxt++; };
    need(G2_N0);
    STAMP_AT(4);
    __builtin_amdgcn_s_setprio(3);
    const int o0 = next(), o1 = next();
    int q2 = next(), q3;
    dmaB(o0, 0); dmaB(o1, 1); dmaB(q2, 2);
#pragma unroll
    for (int i = 0; i < 8; ++i) A[0][i] = ldA(o0, i);
#pragma unroll
    for (int i = 0; i < 8; ++i) A[1][i] = ldA(o1, i);
    f32x4 acc[8][4];
    WAIT_V(16);
#pragma unroll
    for (int n = 0; n < 4; ++n) B[0][n] = ldB(0, n);
    SCHED();
    auto step = [&](const int sq, const bool first, const bool pre3, const bool pre2, const bool pre1) AI {
      const int sl = sq & 1;
      if (pre3) q3 = next();
      WAIT_L(0);
      if (pre3) dmaB(q3, (sq + 3) & 3);
      if (pre1) {
        if (pre3) { WAIT_V(24); } else if (pre2) { WAIT_V(20); } else { WAIT_V(8); }
#pragma unroll
        for (int n = 0; n < 4; ++n) B[sl ^ 1][n] = ldB((sq + 1) & 3, n);
      }
      SCHED();
#pragma unroll
      for (int i = 0; i < 8; ++i) {
#pragma unroll
        for (int n = 0; n < 4; ++n) { if (first) mfma16z(acc[i][n], B[sl][n], A[sl][i]); else mfma16a(acc[i][n], B[sl][n], A[sl][i]); }
        if (pre2) A[sl][i] = ldA(q2, i);
        SCHED();
      }
      q2 = q3;
    };
    step(0, true, true, true, true); step(1, false, true, true, true); step(2, false, true, true, true); step(3, false, true, true, true);
    u64 pv = flag_peek(ws, hfo, 56);
    for (int s = 4; s < 24; s += 4) {
      if (s == 8) poll_eval(pv);
      if (s == 12) { STAMP_AT(5); *(volatile LAS unsigned*)(lds + L_CTRL + 48 + w4 * 4) = 1u; }
      need(s + 7);
      step(0, false, true, true, true); step(1, false, true, true, true); step(2, false, true, true, true); step(3, false, true, true, true);
    }
    need(28);
    step(0, false, true, true, true); step(1, false, false, true, true); step(2, false, false, false, true); step(3, false, false, false, false);
    MFMA_DRAIN();
    STAMP_AT(6);
    lds_wait4(lds + L_CTRL + 32);
#pragma unroll
    for (int o = 0; o < 4; ++o) {
      if (o != w4) {
        const int d = (o < w4) ? o : o - 1;
        LAS char* dst = (d < 2) ? ring + d * 8192 : lds + w4 * CV_SIZE;
#pragma unroll
        for (int i2 = 0; i2 < 2; ++i2)
#pragma unroll
          for (int n = 0; n < 4; ++n) *(LAS f32x4*)(dst + (i2 * 4 + n) * 1024 + lo16) = acc[o * 2 + i2][n];
      }
    }
    f32x4 fin[2][4];
#pragma unroll
    for (int o = 0; o < 4; ++o)
      if (o == w4) {
#pragma unroll
        for (int i2 = 0; i2 < 2; ++i2)
#pragma unroll
          for (int n = 0; n < 4; ++n) fin[i2][n] = acc[o * 2 + i2][n];
      }
    WAIT_L(0);
    BAR();
    asm volatile("" ::: "memory");
#pragma unroll
    for (int k = 1; k < 4; ++k) {
      const int sw = (w4 + k) & 3;
      const int d = (w4 < sw) ? w4 : w4 - 1;
      LAS char* src = (d < 2) ? lds + L_RING + sw * 16384 + d * 8192 : lds + sw * CV_SIZE;
#pragma unroll
      for (int i2 = 0; i2 < 2; ++i2)
#pragma unroll
        for (int n = 0; n < 4; ++n) fin[i2][n] += *(LAS f32x4*)(src + (i2 * 4 + n) * 1024 + lo16);
    }
    LAS char* sg = lds + w4 * CV_SIZE + L_STG;
#pragma unroll
    for (int i2 = 0; i2 < 2; ++i2) {
      const int row = rt * 128 + (w4 * 2 + i2) * 16 + fr;
      const float og = *(LAS float*)(sg + 8192 + i2 * 256 + lane * 4);
      unsigned ow[4][2];
#pragma unroll
      for (int n = 0; n < 4; ++n) {
        const f32x4 ga = *(LAS f32x4*)(sg + (i2 * 4 + n) * 1024 + lo16);
        float r4[4];
#pragma unroll
        for (int r = 0; r < 4; ++r) r4[r] = ga[r] + rbf(og * rbf(fin[i2][n][r]));
        ow[n][0] = pack2(r4[0], r4[1]); ow[n][1] = pack2(r4[2], r4[3]);
      }
#pragma unroll
      for (int n2 = 0; n2 < 2; ++n2) {
        const auto r0 = __builtin_amdgcn_permlane16_swap(ow[2 * n2][0], ow[2 * n2 + 1][0], false, false);
        const auto r1 = __builtin_amdgcn_permlane16_swap(ow[2 * n2][1], ow[2 * n2 + 1][1], false, false);
        *(u32x4*)(out + (size_t)row * D_ + ct * 64 + n2 * 32 + (fq & 1) * 16 + (fq >> 1) * 8) = (u32x4){r0[0], r1[0], r0[1], r1[1]};
      }
    }
    STAMP_AT(7);
  } else {
    const float dv = bflo(dvraw);
    u32x4 qr[4], gx[2][2], gwv[2];
#pragma unroll
    for (int i = 0; i < 4; ++i) qr[i] = *(const u32x4*)(q + (size_t)(rt * 128 + wid * 32 + i * 8 + (lane >> 3)) * D_ + ct * 64 + (lane & 7) * 8);
#pragma unroll
    for (int r = 0; r < 2; ++r) {
      const u16* xr = q + (size_t)(b * 8 + wid * 2 + r) * D_ + lane * 8;
      gx[r][0] = *(const u32x4*)xr; gx[r][1] = *(const u32x4*)(xr + 512);
    }
    gwv[0] = *(const u32x4*)(gw + lane * 8); gwv[1] = *(const u32x4*)(gw + 512 + lane * 8);
    SCHED();
    STAMP_AT(4);
    conv_wave(lds + wid * CV_SIZE, in, dv, rws, ch);
    {
      float sg2[2];
#pragma unroll
      for (int r = 0; r < 2; ++r) {
        float s = 0.f;
#pragma unroll
        for (int e = 0; e < 4; ++e) {
          s += rbf(bflo(gx[r][0][e]) * bflo(gwv[0][e])) + rbf(bfhi(gx[r][0][e]) * bfhi(gwv[0][e]));
          s += rbf(bflo(gx[r][1][e]) * bflo(gwv[1][e])) + rbf(bfhi(gx[r][1][e]) * bfhi(gwv[1][e]));
        }
#pragma unroll
        for (int o = 32; o >= 1; o >>= 1) s += __shfl_xor(s, o);
        sg2[r] = s;
      }
      const float s = rbf((lane & 1) ? sg2[1] : sg2[0]);
      const float g = rbf(1.0f / (1.0f + __expf(-s)));
      __builtin_amdgcn_raw_buffer_store_b32(__float_as_uint(g), rws, WS_G + (b * 8 + wid * 2 + (lane & 1)) * 4, 0, SC1);
    }
    WAIT_V(0);
    flag_set(ws, WS_FLAG + ((lane & 16) ? (unsigned)((b >> 4) * 16 + (lane & 15)) * FL_STRIDE + FL_G + ((b & 15) * 4 + wid) * 8
                                        : (unsigned)((lane & 15) * 16 + (ch >> 6)) * FL_STRIDE + FL_Z + (ch & 63) * 8), tag);
    STAMP_AT(5);
    *(volatile LAS unsigned*)(lds + L_CTRL + 32 + wid * 4) = 1u;
    LAS char* sg = lds + wid * CV_SIZE + L_STG;
    LAS char* zs = lds + wid * CV_SIZE + L_ZS;
#pragma unroll
    for (int i = 0; i < 4; ++i) *(LAS u32x4*)(sg + (i * 8 + (lane >> 3)) * 144 + (lane & 7) * 16) = qr[i];
    WAIT_L(0);
    u32x2 qv[2][4];
#pragma unroll
    for (int i2 = 0; i2 < 2; ++i2)
#pragma unroll
      for (int n = 0; n < 4; ++n) qv[i2][n] = *(LAS u32x2*)(sg + (i2 * 16 + fr) * 144 + n * 32 + fq * 8);
    lds_wait4<16>(lds + L_CTRL + 48);
    {
      const unsigned tl = (unsigned)tag, th = (unsigned)(tag >> 32);
      for (unsigned spins = 0; spins < (1u << 16); ++spins) {
        const u32x4 v = ld16(rws, lane * 16, myfl + FL_G);
        if (__all(v[0] == tl && v[1] == th && v[2] == tl && v[3] == th)) break;
        __builtin_amdgcn_s_sleep(16);
      }
      asm volatile("" ::: "memory");
      __builtin_amdgcn_fence(__ATOMIC_ACQUIRE, "wavefront");
    }
    float gv[2];
    {
      u32x4 zr[4];
#pragma unroll
      for (int i2 = 0; i2 < 2; ++i2) gv[i2] = __uint_as_float(__builtin_amdgcn_raw_buffer_load_b32(rws, (rt * 128 + (wid * 2 + i2) * 16 + fr) * 4, WS_G, SC1));
#pragma unroll
      for (int i = 0; i < 4; ++i) zr[i] = ld16(rws, (unsigned)(ct * 65 + i * 16 + (lane >> 2)) * 4096u + (lane & 3) * 16, WS_ZT + (unsigned)(rt * 128 + wid * 32) * 2u);
#pragma unroll
      for (int i = 0; i < 4; ++i) *(LAS u32x4*)(zs + (i * 16 + (lane >> 2)) * 80 + (lane & 3) * 16) = zr[i];
    }
    WAIT_L(0);
    const int q4 = (lane & 15) >> 2, p = lane & 3;
#pragma unroll
    for (int i2 = 0; i2 < 2; ++i2) {
      const float g = gv[i2];
#pragma unroll
      for (int n = 0; n < 4; ++n) {
        const s16x4 zz = __builtin_amdgcn_ds_read_tr16_b64_v4i16((LAS s16x4*)(zs + (16 * n + 4 * fq + q4) * 80 + (i2 * 16 + 4 * p) * 2));
        const u32x2 zu = __builtin_bit_cast(u32x2, zz);
        const unsigned q01 = qv[i2][n][0], q23 = qv[i2][n][1];
        f32x4 ga;
        ga[0] = rbf(g * rbf(bflo(q01) * bflo(zu[0]))); ga[1] = rbf(g * rbf(bfhi(q01) * bfhi(zu[0])));
        ga[2] = rbf(g * rbf(bflo(q23) * bflo(zu[1]))); ga[3] = rbf(g * rbf(bfhi(q23) * bfhi(zu[1])));
        *(LAS f32x4*)(sg + (i2 * 4 + n) * 1024 + lane * 16) = ga;
      }
      *(LAS float*)(sg + 8192 + i2 * 256 + lane * 4) = rbf(1.0f - g);
    }
    WAIT_L(0);
    STAMP_AT(6);
    BAR();
    if (wid < 3) *(u32x4*)(ws + myfl + (wid * 64 + lane) * 16) = (u32x4){0u, 0u, 0u, 0u};
    STAMP_AT(7);
  }
}

extern "C" void kernel_launch(void* const* d_in, const int* in_sizes, int n_in, void* d_out, int out_size, void* d_ws, size_t ws_size,
                              hipStream_t stream) {
  const u16* q = (const u16*)d_in[0]; const u16* kk = (const u16*)d_in[1]; const u16* vv = (const u16*)d_in[2];
  const u16* shk = (const u16*)d_in[3]; const u16* ssmk = (const u16*)d_in[4]; const u16* ssmD = (const u16*)d_in[5];
  const u16* wg = (const u16*)d_in[7]; const u16* wu = (const u16*)d_in[8]; const u16* wd = (const u16*)d_in[9];
  const u16* gw = (const u16*)d_in[10];
  constexpr int LDS1 = 163840;
  (void)hipFuncSetAttribute((const void*)k1_kernel, hipFuncAttributeMaxDynamicSharedMemorySize, LDS1);
  k1_kernel<<<256, 512, LDS1, stream>>>(q, kk, vv, shk, ssmk, ssmD, wg, wu, wd, gw, (char*)d_ws, (u16*)d_out);
}
```

```cpp
#include <hip/hip_runtime.h>
#include <hip/hip_bf16.h>

typedef unsigned short u16;
typedef unsigned long long u64;
typedef short s16x4 __attribute__((ext_vector_type(4)));
typedef short s16x8 __attribute__((ext_vector_type(8)));
typedef __bf16 bfx2 __attribute__((ext_vector_type(2)));
typedef float f32x4 __attribute__((ext_vector_type(4)));
typedef unsigned u32x4 __attribute__((ext_vector_type(4)));
typedef unsigned u32x2 __attribute__((ext_vector_type(2)));
typedef __amdgpu_buffer_rsrc_t rsrc_t;
#define LAS __attribute__((address_space(3)))
#define GAS __attribute__((address_space(1)))
#define DEV __device__ __forceinline__
#define AI __attribute__((always_inline))
extern "C" __device__ u64 hw_dispatch_id() __asm("llvm.amdgcn.dispatch.id");

constexpr int S_ = 2048, D_ = 1024, F_ = 3584;
constexpr unsigned WS_HF = 0;
constexpr unsigned HF_KS = 129 * 1024;
constexpr unsigned WS_KVT = 16u << 20;
constexpr unsigned WS_ZT = 21u << 20;
constexpr unsigned WS_G = 26u << 20;
constexpr unsigned WS_FLAG = 27u << 20;
constexpr unsigned WS_BYTES = 28u << 20;
__device__ constexpr unsigned ch_off(unsigned ch) { return (ch + (ch >> 6)) * 4096u; }
constexpr int FL_H = 0, FL_G = 1024, FL_Z = 1536, FL_KV = 2048, FL_BYTES = 2560;
constexpr unsigned FL_STRIDE = 4096;
constexpr int SC1 = 16;
constexpr int L_PFS = 163840 - 512, L_CTRL = 163840 - 64;

DEV float bflo(unsigned w) { return __uint_as_float(w << 16); }
DEV float bfhi(unsigned w) { return __uint_as_float(w & 0xffff0000u); }
DEV unsigned pack2(float lo, float hi) { bfx2 p; p[0] = (__bf16)lo; p[1] = (__bf16)hi; return __builtin_bit_cast(unsigned, p); }
DEV float rbf(float f) { return (float)(__bf16)f; }
DEV void mfma16a(f32x4& c, s16x8 a, s16x8 b) {
  asm volatile("v_mfma_f32_16x16x32_bf16 %0, %1, %2, %0" : "+v"(c) : "v"(a), "v"(b));
}
DEV void mfma16z(f32x4& c, s16x8 a, s16x8 b) {
  asm volatile("v_mfma_f32_16x16x32_bf16 %0, %1, %2, 0" : "=&v"(c) : "v"(a), "v"(b));
}
DEV s16x8 tr_read8(LAS char* p0, LAS char* p1) {
  s16x4 lo = __builtin_amdgcn_ds_read_tr16_b64_v4i16((LAS s16x4*)p0);
  s16x4 hi = __builtin_amdgcn_ds_read_tr16_b64_v4i16((LAS s16x4*)p1);
  return __builtin_shufflevector(lo, hi, 0, 1, 2, 3, 4, 5, 6, 7);
}
template <int OFF, int D2>
DEV s16x8 tr_read8_asm(unsigned addr) {
  s16x4 lo, hi;
  asm volatile("ds_read_b64_tr_b16 %0, %2 offset:%3\n\tds_read_b64_tr_b16 %1, %2 offset:%4" : "=&v"(lo), "=&v"(hi) : "v"(addr), "i"(OFF), "i"(OFF + D2));
  return __builtin_shufflevector(lo, hi, 0, 1, 2, 3, 4, 5, 6, 7);
}
template <int OFF>
DEV s16x8 ds_read16_asm(unsigned addr) {
  s16x8 r;
  asm volatile("ds_read_b128 %0, %1 offset:%2" : "=v"(r) : "v"(addr), "i"(OFF));
  return r;
}
template <int V> struct IC { static constexpr int value = V; };
DEV rsrc_t mk_rsrc(const void* p, unsigned bytes) { return __builtin_amdgcn_make_buffer_rsrc((void*)p, 0, bytes, 0x00020000); }
DEV void blds16(rsrc_t r, LAS char* l, unsigned voff, unsigned soff) {
  __builtin_amdgcn_raw_ptr_buffer_load_lds(r, (LAS void*)l, 16, voff, soff, 0, 0);
}
DEV void blds4(rsrc_t r, LAS char* l, unsigned voff, unsigned soff) {
  __builtin_amdgcn_raw_ptr_buffer_load_lds(r, (LAS void*)l, 4, voff, soff, 0, 0);
}
DEV u32x4 ld16(rsrc_t r, unsigned voff, unsigned soff) { return __builtin_amdgcn_raw_buffer_load_b128(r, voff, soff, SC1); }
DEV void st16(rsrc_t r, unsigned voff, u32x4 v) { __builtin_amdgcn_raw_buffer_store_b128(v, r, voff, 0, SC1); }
#define WAIT_V(n) asm volatile("s_waitcnt vmcnt(" #n ")" ::: "memory")
#define WAIT_L(n) asm volatile("s_waitcnt lgkmcnt(" #n ")" ::: "memory")
#define BAR() __builtin_amdgcn_s_barrier()
#define SCHED() __builtin_amdgcn_sched_barrier(0)
#define MFMA_DRAIN() do { asm volatile("s_nop 7\n\ts_nop 7" ::: "memory"); SCHED(); } while (0)

DEV void flag_set(char* ws, unsigned off, u64 tag) { __hip_atomic_store((GAS u64*)(ws + off), tag, __ATOMIC_RELAXED, __HIP_MEMORY_SCOPE_AGENT); }
DEV u64 flag_peek(char* ws, unsigned off, int n) {
  const int lane = threadIdx.x & 63;
  return __hip_atomic_load((GAS u64*)(ws + off + 8 * (lane < n ? lane : 0)), __ATOMIC_RELAXED, __HIP_MEMORY_SCOPE_AGENT);
}
template <int SLP>
DEV void flag_wait(u64 first, char* ws, unsigned off, int n, u64 tag) {
  u64 v = first;
  for (unsigned spins = 0; spins < (1u << 16); ++spins) {
    if (__all(v == tag)) break;
    __builtin_amdgcn_s_sleep(SLP);
    v = flag_peek(ws, off, n);
  }
  __builtin_amdgcn_fence(__ATOMIC_ACQUIRE, "wavefront");
}
template <int SLP = 1>
DEV void lds_wait4(LAS char* p) {
  for (unsigned spins = 0; spins < (1u << 22); ++spins) {
    const u32x4 dn = *(volatile LAS u32x4*)p;
    if (__builtin_amdgcn_readfirstlane(dn[0] & dn[1] & dn[2] & dn[3]) != 0u) break;
    __builtin_amdgcn_s_sleep(SLP);
  }
  asm volatile("" ::: "memory");
}
DEV int tile_of_block(int b) { const int xcd = b & 7, j = b >> 3; return ((xcd >> 1) * 4 + (j >> 3)) * 16 + (xcd & 1) * 8 + (j & 7); }

struct Args {
  const u16 *q, *kk, *vv, *shk, *ssmk, *ssmD, *wg, *wu, *wd, *gw;
  char* ws; u16* out;
};

DEV void gemm1_main(f32x4 (&acc)[4][8], u64& kvpeek, unsigned kvoff, LAS char* lds, const Args& a, int pm, int pn, int im, int in_) {
  char* ws = a.ws;
  const int tid = threadIdx.x, lane = tid & 63;
  const int wid = __builtin_amdgcn_readfirstlane(tid >> 6);
  const int wr = wid >> 1, wc = wid & 1;
  const int fr = lane & 15, fq = lane >> 4;
  unsigned va[2], vb[2];
#pragma unroll
  for (int e = 0; e < 2; ++e) {
    const int rl = lane >> 3, p = lane & 7;
    const int sw = (e << 2) | (lane >> 4);
    va[e] = (unsigned)(rl * 2048 + ((p ^ sw) * 16));
    const int kl = lane >> 4, pb = lane & 15;
    const int swb = (kl | (e << 2)) << 1;
    vb[e] = (unsigned)(kl * (F_ * 2) + (pb ^ swb) * 16);
  }
  const rsrc_t rx = mk_rsrc(a.q, S_ * D_ * 2);
  const rsrc_t rw = mk_rsrc((wid < 4) ? a.wg : a.wu, D_ * F_ * 2);
  const unsigned xa_base = (unsigned)(pm * 256 + wid * 32) * 2048u;
  const unsigned wb_base = (unsigned)((wid & 3) * 16) * (F_ * 2) + pn * 256;
  LAS char* ldsA = lds + wid * 4096;
  LAS char* ldsB = lds + 65536 + wid * 4096;
  auto dma = [&](int kt, int st, int d) AI {
    const int i = d >> 1;
    if (d & 1) blds16(rw, ldsB + st * 32768 + i * 1024, vb[i >> 1], wb_base + (kt * 64 + i * 4) * (F_ * 2));
    else blds16(rx, ldsA + st * 32768 + i * 1024, va[i & 1], xa_base + i * (8 * 2048) + kt * 128);
  };
#pragma unroll
  for (int d = 0; d < 8; ++d) dma(0, 0, d);
  SCHED(); BAR(); SCHED();
#pragma unroll
  for (int d = 0; d < 8; ++d) dma(1, 1, d);
  SCHED();
  rsrc_t rp; unsigned vp, pstep;
  if (wid < 4) {
    rp = mk_rsrc((im >> 1) ? a.wu : a.wg, D_ * F_ * 2);
    vp = (unsigned)(wid * 16 + (lane & 15)) * (F_ * 2) + pn * 256 + (im & 1) * 128; pstep = 64 * F_ * 2;
  } else {
    rp = rx;
    int r = in_ * 37 + (wid - 4) * 10 + ((lane & 15) < 10 ? (lane & 15) : 9); r = r > 255 ? 255 : r;
    vp = (unsigned)(pm * 256 + r) * 2048u; pstep = 128;
  }
  vp += (unsigned)(lane >> 4) * 32u;
  auto pf = [&](int kt) AI { blds4(rp, lds + L_PFS, vp, kt * pstep); };
  const unsigned lds0 = (unsigned)(size_t)lds;
  unsigned bx[2];
#pragma unroll
  for (int ks = 0; ks < 2; ++ks) bx[ks] = lds0 + (wr * 64 + fr) * 128 + (((ks * 4 + fq) ^ (fr >> 1)) * 16);
  unsigned bw[4];
  {
    const int q = (lane & 15) >> 2, p = lane & 3;
    const int sw = (q | ((fq & 1) << 2)) << 1;
#pragma unroll
    for (int j = 0; j < 4; ++j) bw[j] = lds0 + 65536 + (8 * fq + q) * 256 + (((wc * 8 + j * 2 + (p >> 1)) ^ sw) * 16) + (p & 1) * 8;
  }
  auto ldx = [&](auto ST, auto KS, const int i) AI -> s16x8 {
    constexpr int o = decltype(ST)::value * 32768; const unsigned p = bx[decltype(KS)::value];
    return i == 0 ? ds_read16_asm<o>(p) : i == 1 ? ds_read16_asm<o + 2048>(p) : i == 2 ? ds_read16_asm<o + 4096>(p) : ds_read16_asm<o + 6144>(p);
  };
  auto ldw = [&](auto ST, auto KS, const int n) AI -> s16x8 {
    constexpr int o = decltype(ST)::value * 32768 + decltype(KS)::value * 8192;
    return n < 4 ? tr_read8_asm<o, 1024>(bw[n & 3]) : tr_read8_asm<o + 16384, 1024>(bw[n & 3]);
  };
  s16x8 xa[4], xb[4], w[8];
  pf(2);
  WAIT_V(9); BAR(); SCHED();
#pragma unroll
  for (int i = 0; i < 4; ++i) xa[i] = ldx(IC<0>{}, IC<0>{}, i);
#pragma unroll
  for (int n = 0; n < 8; ++n) w[n] = ldw(IC<0>{}, IC<0>{}, n);
  pf(3);
  WAIT_L(0);
  SCHED();

  auto tile_body = [&](auto ST, const int kt, const bool pre1, const bool pre2, const int hook) AI {
    constexpr int st = decltype(ST)::value;
#pragma unroll
    for (int n = 0; n < 8; ++n) {
      if (pre2) { if (n == 2) __builtin_amdgcn_s_setprio(1); if (n == 6) __builtin_amdgcn_s_setprio(0); }
      if (n == 6) WAIT_L(15);
#pragma unroll
      for (int i = 0; i < 4; ++i) {
        if (n == 0) { if (i == 0) WAIT_L(13); if (i == 1) WAIT_L(10); if (i == 2) WAIT_L(7); if (i == 3) WAIT_L(4); }
        if (hook == 1) mfma16z(acc[i][n], w[n], xa[i]); else mfma16a(acc[i][n], w[n], xa[i]);
      }
      w[n] = ldw(ST, IC<1>{}, n);
      if (n < 4) xb[n] = ldx(ST, IC<1>{}, n);
      if (n == 3 && hook <= 1) pf(kt + 4);
      if (n == 3 && hook == 5) kvpeek = flag_peek(ws, kvoff, 32);
      SCHED();
    }
#pragma unroll
    for (int n = 0; n < 2; ++n) {
#pragma unroll
      for (int i = 0; i < 4; ++i) {
        if (n == 0) { if (i == 0) WAIT_L(15); if (i == 1) WAIT_L(14); if (i == 2) WAIT_L(11); if (i == 3) WAIT_L(8); }
        mfma16a(acc[i][n], w[n], xb[i]);
      }
    }
    SCHED();
    WAIT_L(0);
    if (pre1) {
      if (hook == 1) { WAIT_V(3); } else if (hook == 7) { WAIT_V(0); } else { WAIT_V(1); }
      BAR(); SCHED();
      if (pre2) __builtin_amdgcn_s_setprio(3);
      else if (wid < 4) __builtin_amdgcn_s_setprio(2);
      else __builtin_amdgcn_s_setprio(1);
      w[0] = ldw(IC<(st ^ 1)>{}, IC<0>{}, 0); w[1] = ldw(IC<(st ^ 1)>{}, IC<0>{}, 1);
      if (pre2) { dma(kt + 2, st, 0); dma(kt + 2, st, 1); }
      SCHED();
    }
#pragma unroll
    for (int n = 2; n < 8; ++n) {
      if (pre2 && n == 6) __builtin_amdgcn_s_setprio(2);
#pragma unroll
      for (int i = 0; i < 4; ++i) mfma16a(acc[i][n], w[n], xb[i]);
      if (pre1) {
        w[n] = ldw(IC<(st ^ 1)>{}, IC<0>{}, n);
        if (n - 2 < 4) xa[n - 2] = ldx(IC<(st ^ 1)>{}, IC<0>{}, n - 2);
        if (pre2) dma(kt + 2, st, n);
      }
      SCHED();
    }
  };
  tile_body(IC<0>{}, 0, true, true, 1);
  tile_body(IC<1>{}, 1, true, true, 0);
  for (int kt = 2; kt < 12; kt += 2) {
    tile_body(IC<0>{}, kt, true, true, 0);
    tile_body(IC<1>{}, kt + 1, true, true, 0);
  }
  tile_body(IC<0>{}, 12, true, true, 7);
  tile_body(IC<1>{}, 13, true, true, 7);
  tile_body(IC<0>{}, 14, true, false, 5);
  tile_body(IC<1>{}, 15, false, false, 7);
  *(volatile LAS unsigned*)(lds + L_CTRL + wid * 4) = 1u;
  MFMA_DRAIN();
}

DEV void light_unit(LAS char* lds, const Args& a, rsrc_t rws, u64 tag, int cu) {
  char* ws = a.ws;
  const int tid = threadIdx.x, lane = tid & 63;
  const int wid = __builtin_amdgcn_readfirstlane(tid >> 6);
  const int cb = cu & 7, tblk = (cu >> 3) * 8 + wid;
  const int kch = cb * 128 + lane * 2;
  LAS char* wl = lds + wid * (128 * 144);
  const u32x4 shw = *(const u32x4*)(a.shk + kch * 4);
#pragma unroll
  for (int hf = 0; hf < 2; ++hf) {
    const int t0 = tblk * 64 + hf * 32;
    unsigned kr[35], vr[32];
#pragma unroll
    for (int jx = 0; jx < 35; ++jx) { const int t = t0 - 3 + jx; kr[jx] = *(const unsigned*)(a.kk + (size_t)(t < 0 ? 0 : t) * D_ + kch); }
#pragma unroll
    for (int jx = 0; jx < 32; ++jx) vr[jx] = *(const unsigned*)(a.vv + (size_t)(t0 + jx) * D_ + kch);
    SCHED();
#pragma unroll
    for (int c = 0; c < 2; ++c) {
      const unsigned s01 = shw[c * 2], s23 = shw[c * 2 + 1];
      const float w0 = bflo(s01), w1 = bfhi(s01), w2 = bflo(s23), w3 = bfhi(s23);
#pragma unroll
      for (int sb = 0; sb < 4; ++sb) {
        float kf[11];
#pragma unroll
        for (int jx = 0; jx < 11; ++jx) { kf[jx] = c ? bfhi(kr[sb * 8 + jx]) : bflo(kr[sb * 8 + jx]); if (sb == 0 && jx < 3 && t0 == 0) kf[jx] = 0.f; }
        float o[8];
#pragma unroll
        for (int jx = 0; jx < 8; ++jx) o[jx] = (kf[jx] * w3 + kf[jx + 1] * w2 + kf[jx + 2] * w1 + kf[jx + 3] * w0) * (c ? bfhi(vr[sb * 8 + jx]) : bflo(vr[sb * 8 + jx]));
        *(LAS u32x4*)(wl + (lane * 2 + c) * 144 + (hf * 4 + sb) * 16) = (u32x4){pack2(o[7], o[6]), pack2(o[5], o[4]), pack2(o[3], o[2]), pack2(o[1], o[0])};
      }
    }
  }
  WAIT_L(0);
#pragma unroll
  for (int e = 0; e < 16; ++e) {
    const int c = e * 8 + (lane >> 3);
    const u32x4 v = *(LAS u32x4*)(wl + c * 144 + (lane & 7) * 16);
    st16(rws, WS_KVT + ch_off(cb * 128 + c) + tblk * 128 + (lane & 7) * 16, v);
  }
  WAIT_V(0);
  flag_set(ws, WS_FLAG + (unsigned)tile_of_block(cb * 32 + (lane & 31)) * FL_STRIDE + FL_KV + tblk * 8, tag);
  const rsrc_t rd = mk_rsrc(a.wd, F_ * D_ * 2), rk = mk_rsrc(a.ssmk, D_ * S_ * 2);
#pragma unroll
  for (int i = 0; i < 2; ++i) blds4(rk, lds + L_PFS, tid * 128, cu * 131072 + i * 65536);
#pragma unroll
  for (int i = 0; i < 4; ++i) blds4(rd, lds + L_PFS, tid * 128, (i * 32 + cu) * 57344);
  WAIT_V(0);
  __syncthreads();
}

DEV u32x2 geglu4(f32x4 gt, f32x4 up) {
  const float C1 = -2.302208198f, C2 = -2.302208198f * 0.044715f;
  const f32x4 wv = gt * (gt * gt * C2 + C1);
  f32x4 ev;
#pragma unroll
  for (int r = 0; r < 4; ++r) ev[r] = __builtin_amdgcn_exp2f(wv[r]);
  const f32x4 dv = ev + 1.0f;
  f32x4 rv;
#pragma unroll
  for (int r = 0; r < 4; ++r) rv[r] = __builtin_amdgcn_rcpf(dv[r]);
  const f32x4 hv = gt * up * rv;
  return (u32x2){pack2(hv[0], hv[1]), pack2(hv[2], hv[3])};
}
DEV void gemm1_epi(const f32x4 (&acc)[4][8], rsrc_t rws, int pm, int pn) {
  const int lane = threadIdx.x & 63;
  const int wid = __builtin_amdgcn_readfirstlane(threadIdx.x >> 6);
  const int wr = wid >> 1, wc = wid & 1;
  const unsigned off = WS_HF + (unsigned)(pn * 4 + wc * 2) * HF_KS + (unsigned)(pm * 16 + wr * 4) * 1024u + lane * 16;
#pragma unroll
  for (int i = 0; i < 4; ++i)
#pragma unroll
    for (int k2 = 0; k2 < 2; ++k2) {
      const u32x2 h0 = geglu4(acc[i][k2 * 2], acc[i][4 + k2 * 2]), h1 = geglu4(acc[i][k2 * 2 + 1], acc[i][4 + k2 * 2 + 1]);
      st16(rws, off + k2 * HF_KS + i * 1024, (u32x4){h0[0], h0[1], h1[0], h1[1]});
    }
}

constexpr int CV_CS = 4160, CV_U = 4 * CV_CS  , CV_SIZE = 23552;
struct ConvIn { u32x4 own[4]; u32x2 nxt[4], h0; u32x4 u[4]; };

DEV void conv_write_seg(LAS char* wl, int seg, u32x4 a, u32x2 b) {
  const unsigned a01 = __builtin_amdgcn_alignbit(a[1], a[0], 16), a12 = __builtin_amdgcn_alignbit(a[2], a[1], 16),
                 a23 = __builtin_amdgcn_alignbit(a[3], a[2], 16), a34 = __builtin_amdgcn_alignbit(b[0], a[3], 16),
                 a45 = __builtin_amdgcn_alignbit(b[1], b[0], 16);
  LAS char* dst = wl + seg * 16;
  *(LAS u32x4*)(dst) = a;
  *(LAS u32x4*)(dst + CV_CS) = (u32x4){a01, a12, a23, a34};
  *(LAS u32x4*)(dst + 2 * CV_CS) = (u32x4){a[1], a[2], a[3], b[0]};
  *(LAS u32x4*)(dst + 3 * CV_CS) = (u32x4){a12, a23, a34, a45};
}
DEV s16x8 conv_ldp(LAS char* p) {
  volatile LAS s16x4* pp = (volatile LAS s16x4*)p;
  const s16x4 lo = pp[0], hi = pp[1];
  return __builtin_shufflevector(lo, hi, 0, 1, 2, 3, 4, 5, 6, 7);
}

template <class Mid>
DEV void conv_wave(LAS char* wl, const ConvIn& in, float dv, rsrc_t rws, int ch, Mid&& mid) {
  const int lane = threadIdx.x & 63;
  const int fr = lane & 15, fq = lane >> 4;
  const u32x4 zero4 = (u32x4){0u, 0u, 0u, 0u};
#pragma unroll
  for (int p = 0; p < 4; ++p) conv_write_seg(wl, 64 * p + lane + 2, in.own[p], in.nxt[p]);
  conv_write_seg(wl, lane & 1, zero4, (lane & 1) ? in.h0 : (u32x2){0u, 0u});
  *(LAS u32x4*)(wl + CV_U + (lane & 31) * 16) = zero4;
#pragma unroll
  for (int i = 0; i < 4; ++i) *(LAS u32x4*)(wl + CV_U + 512 + i * 1024 + lane * 16) = in.u[i];
  const int o_ = 1 + fr + 8 * fq;
  LAS char* pb = wl + (o_ & 3) * CV_CS + 2 * (o_ & ~3);
  LAS char* qb = wl + CV_U + 16 * (33 + 2 * fr - fq);
  f32x4 acc[8];
#pragma unroll
  for (int g = 0; g < 8; ++g) acc[g] = (f32x4){0.f, 0.f, 0.f, 0.f};
  s16x8 P0[8], Q0[8], P1[8], Q1[8];
  WAIT_L(0); SCHED();
#pragma unroll
  for (int a = 0; a < 8; ++a) { P0[a] = conv_ldp(pb + a * 512); Q0[a] = *(LAS s16x8*)(qb + a * 512); }
  SCHED();
  auto cls = [&](s16x8 (&P)[8], s16x8 (&Q)[8], s16x8 (&Pn)[8], s16x8 (&Qn)[8], LAS char* pn, LAS char* qn) AI {
#pragma unroll
    for (int b = 0; b < 8; ++b) {
      Pn[b] = conv_ldp(pn + b * 512); Qn[b] = *(LAS s16x8*)(qn + b * 512);
#pragma unroll
      for (int a = 0; a + b < 8; ++a) mfma16a(acc[a + b], P[a], Q[b]);
      SCHED();
    }
  };
  for (int r = 0; r < 8; r += 2) {
    if (r == 4) mid();
    cls(P0, Q0, P1, Q1, pb + (r + 1) * 64, qb - (r + 1) * 64);
    const int rn = (r + 2) & 7;
    cls(P1, Q1, P0, Q0, pb + rn * 64, qb - rn * 64);
  }
  MFMA_DRAIN();
#pragma unroll
  for (int e = 0; e < 4; ++e) {
    unsigned zx[2][2];
#pragma unroll
    for (int h = 0; h < 2; ++h) {
      const int g = 2 * e + h;
      const int al = 32 * g + 2 * fr + (fq >> 1);
      const u32x2 kvp = *(LAS u32x2*)(wl + CV_U + 512 + al * 16 + ((fq & 1) ? 0 : 8));
      const float k3 = bflo(kvp[0]), k2 = bfhi(kvp[0]), k1 = bflo(kvp[1]), k0 = bfhi(kvp[1]);
      const float z0 = rbf(acc[g][0]) + rbf(k0 * dv), z1 = rbf(acc[g][1]) + rbf(k1 * dv), z2 = rbf(acc[g][2]) + rbf(k2 * dv), z3 = rbf(acc[g][3]) + rbf(k3 * dv);
      zx[h][0] = pack2(z0, z1); zx[h][1] = pack2(z2, z3);
    }
    const auto r0 = __builtin_amdgcn_permlane16_swap(zx[0][0], zx[1][0], false, false);
    const auto r1 = __builtin_amdgcn_permlane16_swap(zx[0][1], zx[1][1], false, false);
    st16(rws, WS_ZT + ch_off(ch) + (unsigned)(256 * (2 * e + (fq & 1)) + 16 * fr + 8 * (fq >> 1)) * 2u, (u32x4){r0[0], r1[0], r0[1], r1[1]});
  }
}

constexpr int L_STG = 8192, L_ZS = L_STG + 9216, L_RING = 4 * CV_SIZE;
static_assert(L_ZS + 5120 <= CV_SIZE && CV_U + 4608 <= CV_SIZE && L_RING + 65536 <= L_PFS, "lds map");

__global__ __launch_bounds__(512, 2) void k1_kernel(const u16* __restrict__ q, const u16* __restrict__ kk, const u16* __restrict__ vv,
                                                    const u16* __restrict__ shk, const u16* __restrict__ ssmk, const u16* __restrict__ ssmD,
                                                    const u16* __restrict__ wg, const u16* __restrict__ wu, const u16* __restrict__ wd,
                                                    const u16* __restrict__ gw, char* ws, u16* __restrict__ out) {
  extern __shared__ __attribute__((aligned(16))) char shm[];
  LAS char* lds = (LAS char*)shm;
  const int tid = threadIdx.x, lane = tid & 63;
  const int wid = __builtin_amdgcn_readfirstlane(tid >> 6);
  const int fr = lane & 15, fq = lane >> 4;
  const int b = blockIdx.x, xcd = b & 7, j = b >> 3;
  const u64 tag = ((hw_dispatch_id() * 0x9E3779B97F4A7C15ull) ^ 0xD1B54A32D192ED03ull) | 1ull;
  const rsrc_t rws = mk_rsrc(ws, WS_BYTES);
  const int rt = (xcd >> 1) * 4 + (j >> 3), ct = (xcd & 1) * 8 + (j & 7);
  const unsigned myfl = WS_FLAG + (unsigned)(rt * 16 + ct) * FL_STRIDE;
  const int ch = b * 4 + (wid & 3);
  const bool light = j < 4;
  const int jj = light ? 0 : j - 4;
  const int pm = (xcd & 1) * 4 + (jj & 3), pn = (xcd >> 1) * 7 + (jj >> 2);
  const Args a{q, kk, vv, shk, ssmk, ssmD, wg, wu, wd, gw, ws, out};

  if (tid < 16) *(volatile LAS unsigned*)(lds + L_CTRL + tid * 4) = (light && tid < 8) ? 1u : 0u;
  ConvIn in;
  const u16* hrow = ssmk + (size_t)ch * S_;
  unsigned dvraw = 0;
  auto conv_issue_h = [&]() AI {
#pragma unroll
    for (int p = 0; p < 4; ++p) {
      in.own[p] = *(const u32x4*)(hrow + 8 * (64 * p + lane));
      const int nx = 64 * p + lane + 1;
      in.nxt[p] = *(const u32x2*)(hrow + 8 * (nx < 256 ? nx : 255));
    }
    in.h0 = *(const u32x2*)hrow;
    dvraw = ssmD[ch];
  };
  auto conv_issue_u = [&]() AI {
#pragma unroll
    for (int i = 0; i < 4; ++i) in.u[i] = ld16(rws, lane * 16 + i * 1024, WS_KVT + ch_off(ch));
  };
  auto drain_in = [&]() AI {
    asm volatile("s_waitcnt vmcnt(0)" : "+v"(in.u[0]), "+v"(in.u[1]), "+v"(in.u[2]), "+v"(in.u[3]), "+v"(in.own[0]), "+v"(in.own[1]), "+v"(in.own[2]), "+v"(in.own[3]),
                 "+v"(in.nxt[0]), "+v"(in.nxt[1]), "+v"(in.nxt[2]), "+v"(in.nxt[3]), "+v"(in.h0), "+v"(dvraw) :: "memory");
  };
  u64 kv0 = 0;
  if (light) {
    light_unit(lds, a, rws, tag, xcd * 4 + j);
    conv_issue_h();
    flag_wait<4>(kv0, ws, myfl + FL_KV, 32, tag);
    conv_issue_u();
    drain_in();
  } else {
    f32x4 acc1[4][8];
    gemm1_main(acc1, kv0, myfl + FL_KV, lds, a, pm, pn, jj & 3, jj >> 2);
    flag_wait<4>(kv0, ws, myfl + FL_KV, 32, tag);
    conv_issue_h();
    conv_issue_u();
    SCHED();
    gemm1_epi(acc1, rws, pm, pn);
    if (wid < 4) __builtin_amdgcn_s_setprio(0);
    drain_in();
    flag_set(ws, WS_FLAG + (unsigned)((pm * 2 + (wid >> 2)) * 16 + (lane & 15)) * FL_STRIDE + FL_H + (((wid & 1) * 28 + pn) * 2 + ((wid >> 1) & 1)) * 8, tag);
  }
  lds_wait4(lds + L_CTRL); lds_wait4(lds + L_CTRL + 16);

  if (wid >= 4) {
    const int w4 = wid - 4;
    const unsigned lo16 = lane * 16;
    const unsigned ha = WS_HF + (unsigned)w4 * HF_KS + (unsigned)(rt * 8) * 1024u;
    const rsrc_t rd = mk_rsrc(wd, F_ * D_ * 2);
    unsigned vbs[2];
#pragma unroll
    for (int gp = 0; gp < 2; ++gp)
      vbs[gp] = (unsigned)((lane >> 3) & 3) * 2048u + (((lane & 7) ^ ((((lane >> 4) & 1) | (gp << 1)) << 1)) * 16) + (lane >> 5) * (16 * 2048u);
    LAS char* ring = lds + L_RING + w4 * 16384;
    unsigned bb[4];
    {
      const int q4 = (lane & 15) >> 2, p = lane & 3;
      const int f2 = (q4 >> 1) | ((fq & 1) << 1);
#pragma unroll
      for (int n = 0; n < 4; ++n) bb[n] = (unsigned)(size_t)ring + (8 * fq + q4) * 128 + ((((n ^ f2) << 1) + (p >> 1)) * 16) + (p & 1) * 8;
    }
    s16x8 A[2][8], B[2][4];
    auto ldA = [&](int s, int i) AI { return __builtin_bit_cast(s16x8, ld16(rws, lo16 + (i & 3) * 1024, ha + (unsigned)s * (4 * HF_KS) + (i >> 2) * 4096)); };
    auto dmaB = [&](int s, int slot) AI {
#pragma unroll
      for (int g = 0; g < 4; ++g) blds16(rd, ring + slot * 4096 + g * 1024, vbs[g & 1], (unsigned)((4 * s + w4) * 32 + 4 * g) * 2048u + ct * 128);
    };
    auto ldB = [&](const int slot, int n) AI {
      return slot == 0 ? tr_read8_asm<0, 512>(bb[n]) : slot == 1 ? tr_read8_asm<4096, 512>(bb[n]) : slot == 2 ? tr_read8_asm<8192, 512>(bb[n]) : tr_read8_asm<12288, 512>(bb[n]);
    };
    const unsigned hfo = myfl + FL_H + (w4 >> 1) * 448;
    u64 rdy = 0; int nxt = 0;
    auto poll_eval = [&](u64 v) AI {
      rdy = __ballot(v == tag);
      __builtin_amdgcn_fence(__ATOMIC_ACQUIRE, "wavefront");
    };
    auto need = [&](const int upto) AI {
      const u64 mask = (1ull << (2 * upto)) - 1ull;
      for (unsigned spins = 0; (rdy & mask) != mask && spins < (1u << 16); ++spins) {
        if (spins) __builtin_amdgcn_s_sleep(4);
        poll_eval(flag_peek(ws, hfo, 56));
      }
    };
    auto next = [&]() AI -> int { return nxt++; };
    dmaB(0, 0); dmaB(1, 1); dmaB(2, 2);
    need(11);
    WAIT_V(0);
    __builtin_amdgcn_s_setprio(3);
    const int o0 = next(), o1 = next();
    int q2 = next(), q3;
#pragma unroll
    for (int i = 0; i < 8; ++i) A[0][i] = ldA(o0, i);
#pragma unroll
    for (int i = 0; i < 8; ++i) A[1][i] = ldA(o1, i);
    f32x4 acc[8][4];
#pragma unroll
    for (int n = 0; n < 4; ++n) B[0][n] = ldB(0, n);
    SCHED();
    auto step = [&](const int sq, const bool first, const bool pre3, const bool pre2, const bool pre1) AI {
      const int sl = sq & 1;
      if (pre3) q3 = next();
      WAIT_L(0);
      if (pre3) dmaB(q3, (sq + 3) & 3);
      if (pre1) {
        if (pre3) { WAIT_V(24); } else if (pre2) { WAIT_V(20); } else { WAIT_V(8); }
#pragma unroll
        for (int n = 0; n < 4; ++n) B[sl ^ 1][n] = ldB((sq + 1) & 3, n);
      }
      SCHED();
#pragma unroll
      for (int i = 0; i < 8; ++i) {
#pragma unroll
        for (int n = 0; n < 4; ++n) { if (first) mfma16z(acc[i][n], B[sl][n], A[sl][i]); else mfma16a(acc[i][n], B[sl][n], A[sl][i]); }
        if (pre2) A[sl][i] = ldA(q2, i);
        SCHED();
      }
      q2 = q3;
    };
    step(0, true, true, true, true); step(1, false, true, true, true); step(2, false, true, true, true); step(3, false, true, true, true);
    u64 pv = flag_peek(ws, hfo, 56);
    for (int s = 4; s < 24; s += 4) {
      if (s == 8) poll_eval(pv);
      if (s == 20) *(volatile LAS unsigned*)(lds + L_CTRL + 48 + w4 * 4) = 1u;
      need(s + 7);
      step(0, false, true, true, true); step(1, false, true, true, true); step(2, false, true, true, true); step(3, false, true, true, true);
    }
    need(28);
    step(0, false, true, true, true); step(1, false, false, true, true); step(2, false, false, false, true); step(3, false, false, false, false);
    MFMA_DRAIN();
    lds_wait4(lds + L_CTRL + 32);
#pragma unroll
    for (int o = 0; o < 4; ++o) {
      if (o != w4) {
        const int d = (o < w4) ? o : o - 1;
        LAS char* dst = (d < 2) ? ring + d * 8192 : lds + w4 * CV_SIZE;
#pragma unroll
        for (int i2 = 0; i2 < 2; ++i2)
#pragma unroll
          for (int n = 0; n < 4; ++n) *(LAS f32x4*)(dst + (i2 * 4 + n) * 1024 + lo16) = acc[o * 2 + i2][n];
      }
    }
    f32x4 fin[2][4];
#pragma unroll
    for (int o = 0; o < 4; ++o)
      if (o == w4) {
#pragma unroll
        for (int i2 = 0; i2 < 2; ++i2)
#pragma unroll
          for (int n = 0; n < 4; ++n) fin[i2][n] = acc[o * 2 + i2][n];
      }
    WAIT_L(0);
    BAR();
    asm volatile("" ::: "memory");
#pragma unroll
    for (int k = 1; k < 4; ++k) {
      const int sw = (w4 + k) & 3;
      const int d = (w4 < sw) ? w4 : w4 - 1;
      LAS char* src = (d < 2) ? lds + L_RING + sw * 16384 + d * 8192 : lds + sw * CV_SIZE;
#pragma unroll
      for (int i2 = 0; i2 < 2; ++i2)
#pragma unroll
        for (int n = 0; n < 4; ++n) fin[i2][n] += *(LAS f32x4*)(src + (i2 * 4 + n) * 1024 + lo16);
    }
    LAS char* sg = lds + w4 * CV_SIZE + L_STG;
#pragma unroll
    for (int i2 = 0; i2 < 2; ++i2) {
      const int row = rt * 128 + (w4 * 2 + i2) * 16 + fr;
      const float og = *(LAS float*)(sg + 8192 + i2 * 256 + lane * 4);
      unsigned ow[4][2];
#pragma unroll
      for (int n = 0; n < 4; ++n) {
        const f32x4 ga = *(LAS f32x4*)(sg + (i2 * 4 + n) * 1024 + lo16);
        float r4[4];
#pragma unroll
        for (int r = 0; r < 4; ++r) r4[r] = ga[r] + rbf(og * rbf(fin[i2][n][r]));
        ow[n][0] = pack2(r4[0], r4[1]); ow[n][1] = pack2(r4[2], r4[3]);
      }
#pragma unroll
      for (int n2 = 0; n2 < 2; ++n2) {
        const auto r0 = __builtin_amdgcn_permlane16_swap(ow[2 * n2][0], ow[2 * n2 + 1][0], false, false);
        const auto r1 = __builtin_amdgcn_permlane16_swap(ow[2 * n2][1], ow[2 * n2 + 1][1], false, false);
        *(u32x4*)(out + (size_t)row * D_ + ct * 64 + n2 * 32 + (fq & 1) * 16 + (fq >> 1) * 8) = (u32x4){r0[0], r1[0], r0[1], r1[1]};
      }
    }
  } else {
    const float dv = bflo(dvraw);
    u32x4 qr[4], gx[2][2], gwv[2];
#pragma unroll
    for (int i = 0; i < 4; ++i) qr[i] = (u32x4){0u, 0u, 0u, 0u};
    gx[0][0] = gx[0][1] = gx[1][0] = gx[1][1] = gwv[0] = gwv[1] = (u32x4){0u, 0u, 0u, 0u};
    auto side_loads = [&]() AI {
#pragma unroll
    for (int i = 0; i < 4; ++i) qr[i] = *(const u32x4*)(q + (size_t)(rt * 128 + wid * 32 + i * 8 + (lane >> 3)) * D_ + ct * 64 + (lane & 7) * 8);
#pragma unroll
    for (int r = 0; r < 2; ++r) {
      const u16* xr = q + (size_t)(b * 8 + wid * 2 + r) * D_ + lane * 8;
      gx[r][0] = *(const u32x4*)xr; gx[r][1] = *(const u32x4*)(xr + 512);
    }
    gwv[0] = *(const u32x4*)(gw + lane * 8); gwv[1] = *(const u32x4*)(gw + 512 + lane * 8);
    };
    SCHED();
    conv_wave(lds + wid * CV_SIZE, in, dv, rws, ch, side_loads);
    {
      float sg2[2];
#pragma unroll
      for (int r = 0; r < 2; ++r) {
        float s = 0.f;
#pragma unroll
        for (int e = 0; e < 4; ++e) {
          s += rbf(bflo(gx[r][0][e]) * bflo(gwv[0][e])) + rbf(bfhi(gx[r][0][e]) * bfhi(gwv[0][e]));
          s += rbf(bflo(gx[r][1][e]) * bflo(gwv[1][e])) + rbf(bfhi(gx[r][1][e]) * bfhi(gwv[1][e]));
        }
#pragma unroll
        for (int o = 32; o >= 1; o >>= 1) s += __shfl_xor(s, o);
        sg2[r] = s;
      }
      const float s = rbf((lane & 1) ? sg2[1] : sg2[0]);
      const float g = rbf(1.0f / (1.0f + __expf(-s)));
      __builtin_amdgcn_raw_buffer_store_b32(__float_as_uint(g), rws, WS_G + (b * 8 + wid * 2 + (lane & 1)) * 4, 0, SC1);
    }
    WAIT_V(0);
    flag_set(ws, WS_FLAG + ((lane & 16) ? (unsigned)((b >> 4) * 16 + (lane & 15)) * FL_STRIDE + FL_G + ((b & 15) * 4 + wid) * 8
                                        : (unsigned)((lane & 15) * 16 + (ch >> 6)) * FL_STRIDE + FL_Z + (ch & 63) * 8), tag);
    *(volatile LAS unsigned*)(lds + L_CTRL + 32 + wid * 4) = 1u;
    LAS char* sg = lds + wid * CV_SIZE + L_STG;
    LAS char* zs = lds + wid * CV_SIZE + L_ZS;
#pragma unroll
    for (int i = 0; i < 4; ++i) *(LAS u32x4*)(sg + (i * 8 + (lane >> 3)) * 144 + (lane & 7) * 16) = qr[i];
    WAIT_L(0);
    u32x2 qv[2][4];
#pragma unroll
    for (int i2 = 0; i2 < 2; ++i2)
#pragma unroll
      for (int n = 0; n < 4; ++n) qv[i2][n] = *(LAS u32x2*)(sg + (i2 * 16 + fr) * 144 + n * 32 + fq * 8);
    lds_wait4<16>(lds + L_CTRL + 48);
    {
      const unsigned tl = (unsigned)tag, th = (unsigned)(tag >> 32);
      for (unsigned spins = 0; spins < (1u << 16); ++spins) {
        const u32x4 v = ld16(rws, lane * 16, myfl + FL_G);
        if (__all(v[0] == tl && v[1] == th && v[2] == tl && v[3] == th)) break;
        __builtin_amdgcn_s_sleep(16);
      }
      asm volatile("" ::: "memory");
      __builtin_amdgcn_fence(__ATOMIC_ACQUIRE, "wavefront");
    }
    float gv[2];
    {
      u32x4 zr[4];
#pragma unroll
      for (int i2 = 0; i2 < 2; ++i2) gv[i2] = __uint_as_float(__builtin_amdgcn_raw_buffer_load_b32(rws, (rt * 128 + (wid * 2 + i2) * 16 + fr) * 4, WS_G, SC1));
#pragma unroll
      for (int i = 0; i < 4; ++i) zr[i] = ld16(rws, (unsigned)(ct * 65 + i * 16 + (lane >> 2)) * 4096u + (lane & 3) * 16, WS_ZT + (unsigned)(rt * 128 + wid * 32) * 2u);
#pragma unroll
      for (int i = 0; i < 4; ++i) *(LAS u32x4*)(zs + (i * 16 + (lane >> 2)) * 80 + (lane & 3) * 16) = zr[i];
    }
    WAIT_L(0);
    const int q4 = (lane & 15) >> 2, p = lane & 3;
#pragma unroll
    for (int i2 = 0; i2 < 2; ++i2) {
      const float g = gv[i2];
#pragma unroll
      for (int n = 0; n < 4; ++n) {
        const s16x4 zz = __builtin_amdgcn_ds_read_tr16_b64_v4i16((LAS s16x4*)(zs + (16 * n + 4 * fq + q4) * 80 + (i2 * 16 + 4 * p) * 2));
        const u32x2 zu = __builtin_bit_cast(u32x2, zz);
        const unsigned q01 = qv[i2][n][0], q23 = qv[i2][n][1];
        f32x4 ga;
        ga[0] = rbf(g * rbf(bflo(q01) * bflo(zu[0]))); ga[1] = rbf(g * rbf(bfhi(q01) * bfhi(zu[0])));
        ga[2] = rbf(g * rbf(bflo(q23) * bflo(zu[1]))); ga[3] = rbf(g * rbf(bfhi(q23) * bfhi(zu[1])));
        *(LAS f32x4*)(sg + (i2 * 4 + n) * 1024 + lane * 16) = ga;
      }
      *(LAS float*)(sg + 8192 + i2 * 256 + lane * 4) = rbf(1.0f - g);
    }
    WAIT_L(0);
    BAR();
    if (wid < 3) *(u32x4*)(ws + myfl + (wid * 64 + lane) * 16) = (u32x4){0u, 0u, 0u, 0u};
  }
}

extern "C" void kernel_launch(void* const* d_in, const int* in_sizes, int n_in, void* d_out, int out_size, void* d_ws, size_t ws_size,
                              hipStream_t stream) {
  const u16* q = (const u16*)d_in[0]; const u16* kk = (const u16*)d_in[1]; const u16* vv = (const u16*)d_in[2];
  const u16* shk = (const u16*)d_in[3]; const u16* ssmk = (const u16*)d_in[4]; const u16* ssmD = (const u16*)d_in[5];
  const u16* wg = (const u16*)d_in[7]; const u16* wu = (const u16*)d_in[8]; const u16* wd = (const u16*)d_in[9];
  const u16* gw = (const u16*)d_in[10];
  constexpr int LDS1 = 163840;
  (void)hipFuncSetAttribute((const void*)k1_kernel, hipFuncAttributeMaxDynamicSharedMemorySize, LDS1);
  k1_kernel<<<256, 512, LDS1, stream>>>(q, kk, vv, shk, ssmk, ssmD, wg, wu, wd, gw, (char*)d_ws, (u16*)d_out);
}
```

```cpp
#include <hip/hip_runtime.h>
#include <hip/hip_bf16.h>

typedef unsigned short u16;
typedef unsigned long long u64;
typedef short s16x4 __attribute__((ext_vector_type(4)));
typedef short s16x8 __attribute__((ext_vector_type(8)));
typedef __bf16 bfx2 __attribute__((ext_vector_type(2)));
typedef float f32x4 __attribute__((ext_vector_type(4)));
typedef unsigned u32x4 __attribute__((ext_vector_type(4)));
typedef unsigned u32x2 __attribute__((ext_vector_type(2)));
typedef __amdgpu_buffer_rsrc_t rsrc_t;
#define LAS __attribute__((address_space(3)))
#define GAS __attribute__((address_space(1)))
#define DEV __device__ __forceinline__
#define AI __attribute__((always_inline))
extern "C" __device__ u64 hw_dispatch_id() __asm("llvm.amdgcn.dispatch.id");

constexpr int S_ = 2048, D_ = 1024, F_ = 3584;
constexpr unsigned WS_HF = 0;
constexpr unsigned HF_KS = 129 * 1024;
constexpr unsigned WS_KVT = 16u << 20;
constexpr unsigned WS_ZT = 21u << 20;
constexpr unsigned WS_G = 26u << 20;
constexpr unsigned WS_FLAG = 27u << 20;
constexpr unsigned WS_BYTES = 28u << 20;
__device__ constexpr unsigned ch_off(unsigned ch) { return (ch + (ch >> 6)) * 4096u; }
constexpr int FL_H = 0, FL_G = 1024, FL_Z = 1536, FL_KV = 2048, FL_BYTES = 2560;
constexpr unsigned FL_STRIDE = 4096;
constexpr int SC1 = 16;
constexpr int L_PFS = 163840 - 512, L_CTRL = 163840 - 64;

DEV float bflo(unsigned w) { return __uint_as_float(w << 16); }
DEV float bfhi(unsigned w) { return __uint_as_float(w & 0xffff0000u); }
DEV unsigned pack2(float lo, float hi) { bfx2 p; p[0] = (__bf16)lo; p[1] = (__bf16)hi; return __builtin_bit_cast(unsigned, p); }
DEV float rbf(float f) { return (float)(__bf16)f; }
DEV void mfma16a(f32x4& c, s16x8 a, s16x8 b) {
  asm volatile("v_mfma_f32_16x16x32_bf16 %0, %1, %2, %0" : "+v"(c) : "v"(a), "v"(b));
}
DEV void mfma16z(f32x4& c, s16x8 a, s16x8 b) {
  asm volatile("v_mfma_f32_16x16x32_bf16 %0, %1, %2, 0" : "=&v"(c) : "v"(a), "v"(b));
}
DEV s16x8 tr_read8(LAS char* p0, LAS char* p1) {
  s16x4 lo = __builtin_amdgcn_ds_read_tr16_b64_v4i16((LAS s16x4*)p0);
  s16x4 hi = __builtin_amdgcn_ds_read_tr16_b64_v4i16((LAS s16x4*)p1);
  return __builtin_shufflevector(lo, hi, 0, 1, 2, 3, 4, 5, 6, 7);
}
template <int OFF, int D2>
DEV s16x8 tr_read8_asm(unsigned addr) {
  s16x4 lo, hi;
  asm volatile("ds_read_b64_tr_b16 %0, %2 offset:%3\n\tds_read_b64_tr_b16 %1, %2 offset:%4" : "=&v"(lo), "=&v"(hi) : "v"(addr), "i"(OFF), "i"(OFF + D2));
  return __builtin_shufflevector(lo, hi, 0, 1, 2, 3, 4, 5, 6, 7);
}
template <int OFF>
DEV s16x8 ds_read16_asm(unsigned addr) {
  s16x8 r;
  asm volatile("ds_read_b128 %0, %1 offset:%2" : "=v"(r) : "v"(addr), "i"(OFF));
  return r;
}
template <int V> struct IC { static constexpr int value = V; };
DEV rsrc_t mk_rsrc(const void* p, unsigned bytes) { return __builtin_amdgcn_make_buffer_rsrc((void*)p, 0, bytes, 0x00020000); }
DEV void blds16(rsrc_t r, LAS char* l, unsigned voff, unsigned soff) {
  __builtin_amdgcn_raw_ptr_buffer_load_lds(r, (LAS void*)l, 16, voff, soff, 0, 0);
}
DEV void blds4(rsrc_t r, LAS char* l, unsigned voff, unsigned soff) {
  __builtin_amdgcn_raw_ptr_buffer_load_lds(r, (LAS void*)l, 4, voff, soff, 0, 0);
}
DEV u32x4 ld16(rsrc_t r, unsigned voff, unsigned soff) { return __builtin_amdgcn_raw_buffer_load_b128(r, voff, soff, SC1); }
DEV void st16(rsrc_t r, unsigned voff, u32x4 v) { __builtin_amdgcn_raw_buffer_store_b128(v, r, voff, 0, SC1); }
#define WAIT_V(n) asm volatile("s_waitcnt vmcnt(" #n ")" ::: "memory")
#define WAIT_L(n) asm volatile("s_waitcnt lgkmcnt(" #n ")" ::: "memory")
#define BAR() __builtin_amdgcn_s_barrier()
#define SCHED() __builtin_amdgcn_sched_barrier(0)
#define MFMA_DRAIN() do { asm volatile("s_nop 7\n\ts_nop 7" ::: "memory"); SCHED(); } while (0)

DEV void flag_set(char* ws, unsigned off, u64 tag) { __hip_atomic_store((GAS u64*)(ws + off), tag, __ATOMIC_RELAXED, __HIP_MEMORY_SCOPE_AGENT); }
DEV u64 flag_peek(char* ws, unsigned off, int n) {
  const int lane = threadIdx.x & 63;
  return __hip_atomic_load((GAS u64*)(ws + off + 8 * (lane < n ? lane : 0)), __ATOMIC_RELAXED, __HIP_MEMORY_SCOPE_AGENT);
}
template <int SLP>
DEV void flag_wait(u64 first, char* ws, unsigned off, int n, u64 tag) {
  u64 v = first;
  for (unsigned spins = 0; spins < (1u << 16); ++spins) {
    if (__all(v == tag)) break;
    __builtin_amdgcn_s_sleep(SLP);
    v = flag_peek(ws, off, n);
  }
  __builtin_amdgcn_fence(__ATOMIC_ACQUIRE, "wavefront");
}
template <int SLP = 1>
DEV void lds_wait4(LAS char* p) {
  for (unsigned spins = 0; spins < (1u << 22); ++spins) {
    const u32x4 dn = *(volatile LAS u32x4*)p;
    if (__builtin_amdgcn_readfirstlane(dn[0] & dn[1] & dn[2] & dn[3]) != 0u) break;
    __builtin_amdgcn_s_sleep(SLP);
  }
  asm volatile("" ::: "memory");
}
DEV int tile_of_block(int b) { const int xcd = b & 7, j = b >> 3; return ((xcd >> 1) * 4 + (j >> 3)) * 16 + (xcd & 1) * 8 + (j & 7); }

struct Args {
  const u16 *q, *kk, *vv, *shk, *ssmk, *ssmD, *wg, *wu, *wd, *gw;
  char* ws; u16* out;
};

DEV void gemm1_main(f32x4 (&acc)[4][8], u64& kvpeek, unsigned kvoff, LAS char* lds, const Args& a, int pm, int pn, int im, int in_) {
  char* ws = a.ws;
  const int tid = threadIdx.x, lane = tid & 63;
  const int wid = __builtin_amdgcn_readfirstlane(tid >> 6);
  const int wr = wid >> 1, wc = wid & 1;
  const int fr = lane & 15, fq = lane >> 4;
  unsigned va[2], vb[2];
#pragma unroll
  for (int e = 0; e < 2; ++e) {
    const int rl = lane >> 3, p = lane & 7;
    const int sw = (e << 2) | (lane >> 4);
    va[e] = (unsigned)(rl * 2048 + ((p ^ sw) * 16));
    const int kl = lane >> 4, pb = lane & 15;
    const int swb = (kl | (e << 2)) << 1;
    vb[e] = (unsigned)(kl * (F_ * 2) + (pb ^ swb) * 16);
  }
  const rsrc_t rx = mk_rsrc(a.q, S_ * D_ * 2);
  const rsrc_t rw = mk_rsrc((wid < 4) ? a.wg : a.wu, D_ * F_ * 2);
  const unsigned xa_base = (unsigned)(pm * 256 + wid * 32) * 2048u;
  const unsigned wb_base = (unsigned)((wid & 3) * 16) * (F_ * 2) + pn * 256;
  LAS char* ldsA = lds + wid * 4096;
  LAS char* ldsB = lds + 65536 + wid * 4096;
  auto dma = [&](int kt, int st, int d) AI {
    const int i = d >> 1;
    if (d & 1) blds16(rw, ldsB + st * 32768 + i * 1024, vb[i >> 1], wb_base + (kt * 64 + i * 4) * (F_ * 2));
    else blds16(rx, ldsA + st * 32768 + i * 1024, va[i & 1], xa_base + i * (8 * 2048) + kt * 128);
  };
#pragma unroll
  for (int d = 0; d < 8; ++d) dma(0, 0, d);
  SCHED(); BAR(); SCHED();
#pragma unroll
  for (int d = 0; d < 8; ++d) dma(1, 1, d);
  SCHED();
  rsrc_t rp; unsigned vp, pstep;
  if (wid < 4) {
    rp = mk_rsrc((im >> 1) ? a.wu : a.wg, D_ * F_ * 2);
    vp = (unsigned)(wid * 16 + (lane & 15)) * (F_ * 2) + pn * 256 + (im & 1) * 128; pstep = 64 * F_ * 2;
  } else {
    rp = rx;
    int r = in_ * 37 + (wid - 4) * 10 + ((lane & 15) < 10 ? (lane & 15) : 9); r = r > 255 ? 255 : r;
    vp = (unsigned)(pm * 256 + r) * 2048u; pstep = 128;
  }
  vp += (unsigned)(lane >> 4) * 32u;
  auto pf = [&](int kt) AI { blds4(rp, lds + L_PFS, vp, kt * pstep); };
  const unsigned lds0 = (unsigned)(size_t)lds;
  unsigned bx[2];
#pragma unroll
  for (int ks = 0; ks < 2; ++ks) bx[ks] = lds0 + (wr * 64 + fr) * 128 + (((ks * 4 + fq) ^ (fr >> 1)) * 16);
  unsigned bw[4];
  {
    const int q = (lane & 15) >> 2, p = lane & 3;
    const int sw = (q | ((fq & 1) << 2)) << 1;
#pragma unroll
    for (int j = 0; j < 4; ++j) bw[j] = lds0 + 65536 + (8 * fq + q) * 256 + (((wc * 8 + j * 2 + (p >> 1)) ^ sw) * 16) + (p & 1) * 8;
  }
  auto ldx = [&](auto ST, auto KS, const int i) AI -> s16x8 {
    constexpr int o = decltype(ST)::value * 32768; const unsigned p = bx[decltype(KS)::value];
    return i == 0 ? ds_read16_asm<o>(p) : i == 1 ? ds_read16_asm<o + 2048>(p) : i == 2 ? ds_read16_asm<o + 4096>(p) : ds_read16_asm<o + 6144>(p);
  };
  auto ldw = [&](auto ST, auto KS, const int n) AI -> s16x8 {
    constexpr int o = decltype(ST)::value * 32768 + decltype(KS)::value * 8192;
    return n < 4 ? tr_read8_asm<o, 1024>(bw[n & 3]) : tr_read8_asm<o + 16384, 1024>(bw[n & 3]);
  };
  s16x8 xa[4], xb[4], w[8];
  pf(2);
  WAIT_V(9); BAR(); SCHED();
#pragma unroll
  for (int i = 0; i < 4; ++i) xa[i] = ldx(IC<0>{}, IC<0>{}, i);
#pragma unroll
  for (int n = 0; n < 8; ++n) w[n] = ldw(IC<0>{}, IC<0>{}, n);
  pf(3);
  WAIT_L(0);
  SCHED();

  auto tile_body = [&](auto ST, const int kt, const bool pre1, const bool pre2, const int hook) AI {
    constexpr int st = decltype(ST)::value;
#pragma unroll
    for (int n = 0; n < 8; ++n) {
      if (pre2) { if (n == 2) __builtin_amdgcn_s_setprio(1); if (n == 6) __builtin_amdgcn_s_setprio(0); }
      if (n == 6) WAIT_L(15);
#pragma unroll
      for (int i = 0; i < 4; ++i) {
        if (n == 0) { if (i == 0) WAIT_L(13); if (i == 1) WAIT_L(10); if (i == 2) WAIT_L(7); if (i == 3) WAIT_L(4); }
        if (hook == 1) mfma16z(acc[i][n], w[n], xa[i]); else mfma16a(acc[i][n], w[n], xa[i]);
      }
      w[n] = ldw(ST, IC<1>{}, n);
      if (n < 4) xb[n] = ldx(ST, IC<1>{}, n);
      if (n == 3 && hook <= 1) pf(kt + 4);
      if (n == 3 && hook == 5) kvpeek = flag_peek(ws, kvoff, 32);
      SCHED();
    }
#pragma unroll
    for (int n = 0; n < 2; ++n) {
#pragma unroll
      for (int i = 0; i < 4; ++i) {
        if (n == 0) { if (i == 0) WAIT_L(15); if (i == 1) WAIT_L(14); if (i == 2) WAIT_L(11); if (i == 3) WAIT_L(8); }
        mfma16a(acc[i][n], w[n], xb[i]);
      }
    }
    SCHED();
    WAIT_L(0);
    if (pre1) {
      if (hook == 1) { WAIT_V(3); } else if (hook == 7) { WAIT_V(0); } else { WAIT_V(1); }
      BAR(); SCHED();
      if (pre2) __builtin_amdgcn_s_setprio(3);
      else if (wid < 4) __builtin_amdgcn_s_setprio(2);
      else __builtin_amdgcn_s_setprio(1);
      w[0] = ldw(IC<(st ^ 1)>{}, IC<0>{}, 0); w[1] = ldw(IC<(st ^ 1)>{}, IC<0>{}, 1);
      if (pre2) { dma(kt + 2, st, 0); dma(kt + 2, st, 1); }
      SCHED();
    }
#pragma unroll
    for (int n = 2; n < 8; ++n) {
      if (pre2 && n == 6) __builtin_amdgcn_s_setprio(2);
#pragma unroll
      for (int i = 0; i < 4; ++i) mfma16a(acc[i][n], w[n], xb[i]);
      if (pre1) {
        w[n] = ldw(IC<(st ^ 1)>{}, IC<0>{}, n);
        if (n - 2 < 4) xa[n - 2] = ldx(IC<(st ^ 1)>{}, IC<0>{}, n - 2);
        if (pre2) dma(kt + 2, st, n);
      }
      SCHED();
    }
  };
  tile_body(IC<0>{}, 0, true, true, 1);
  tile_body(IC<1>{}, 1, true, true, 0);
  for (int kt = 2; kt < 12; kt += 2) {
    tile_body(IC<0>{}, kt, true, true, 0);
    tile_body(IC<1>{}, kt + 1, true, true, 0);
  }
  tile_body(IC<0>{}, 12, true, true, 7);
  tile_body(IC<1>{}, 13, true, true, 7);
  tile_body(IC<0>{}, 14, true, false, 5);
  tile_body(IC<1>{}, 15, false, false, 7);
  *(volatile LAS unsigned*)(lds + L_CTRL + wid * 4) = 1u;
  MFMA_DRAIN();
}

DEV void light_unit(LAS char* lds, const Args& a, rsrc_t rws, u64 tag, int cu) {
  char* ws = a.ws;
  const int tid = threadIdx.x, lane = tid & 63;
  const int wid = __builtin_amdgcn_readfirstlane(tid >> 6);
  const int cb = cu & 7, tblk = (cu >> 3) * 8 + wid;
  const int kch = cb * 128 + lane * 2;
  LAS char* wl = lds + wid * (128 * 144);
  {
    const int hj = cu & 3;
    if (wid == 7) {
      if (hj == 0) { blds4(mk_rsrc(a.wg, D_ * F_ * 2), lds + L_PFS, lane * 65536, 0); blds4(mk_rsrc(a.wg, D_ * F_ * 2), lds + L_PFS, lane * 65536, 64 * 65536); blds4(mk_rsrc(a.q, S_ * D_ * 2), lds + L_PFS, lane * 65536, 0); }
      if (hj == 1) { blds4(mk_rsrc(a.wu, D_ * F_ * 2), lds + L_PFS, lane * 65536, 0); blds4(mk_rsrc(a.wu, D_ * F_ * 2), lds + L_PFS, lane * 65536, 64 * 65536); blds4(mk_rsrc(a.out, S_ * D_ * 2), lds + L_PFS, lane * 65536, 0); }
      if (hj == 2) { blds4(mk_rsrc(a.wd, D_ * F_ * 2), lds + L_PFS, lane * 65536, 0); blds4(mk_rsrc(a.wd, D_ * F_ * 2), lds + L_PFS, lane * 65536, 64 * 65536); blds4(mk_rsrc(a.ssmk, S_ * D_ * 2), lds + L_PFS, lane * 65536, 0); }
      if (hj == 3) {
        blds4(rws, lds + L_PFS, lane * HF_KS + 131072, WS_HF); blds4(rws, lds + L_PFS, (lane < 48 ? lane : 47) * HF_KS + 131072, WS_HF + 64 * HF_KS);
        blds4(rws, lds + L_PFS, (lane & 15) * (65 * 4096u) + 64 * 4096u + ((lane & 16) ? WS_ZT : WS_KVT), 0);
        blds4(rws, lds + L_PFS, (lane & 15) * 65536 + 4032, WS_FLAG);
      }
    }
  }
  const u32x4 shw = *(const u32x4*)(a.shk + kch * 4);
#pragma unroll
  for (int hf = 0; hf < 2; ++hf) {
    const int t0 = tblk * 64 + hf * 32;
    unsigned kr[35], vr[32];
#pragma unroll
    for (int jx = 0; jx < 35; ++jx) { const int t = t0 - 3 + jx; kr[jx] = *(const unsigned*)(a.kk + (size_t)(t < 0 ? 0 : t) * D_ + kch); }
#pragma unroll
    for (int jx = 0; jx < 32; ++jx) vr[jx] = *(const unsigned*)(a.vv + (size_t)(t0 + jx) * D_ + kch);
    SCHED();
#pragma unroll
    for (int c = 0; c < 2; ++c) {
      const unsigned s01 = shw[c * 2], s23 = shw[c * 2 + 1];
      const float w0 = bflo(s01), w1 = bfhi(s01), w2 = bflo(s23), w3 = bfhi(s23);
#pragma unroll
      for (int sb = 0; sb < 4; ++sb) {
        float kf[11];
#pragma unroll
        for (int jx = 0; jx < 11; ++jx) { kf[jx] = c ? bfhi(kr[sb * 8 + jx]) : bflo(kr[sb * 8 + jx]); if (sb == 0 && jx < 3 && t0 == 0) kf[jx] = 0.f; }
        float o[8];
#pragma unroll
        for (int jx = 0; jx < 8; ++jx) o[jx] = (kf[jx] * w3 + kf[jx + 1] * w2 + kf[jx + 2] * w1 + kf[jx + 3] * w0) * (c ? bfhi(vr[sb * 8 + jx]) : bflo(vr[sb * 8 + jx]));
        *(LAS u32x4*)(wl + (lane * 2 + c) * 144 + (hf * 4 + sb) * 16) = (u32x4){pack2(o[7], o[6]), pack2(o[5], o[4]), pack2(o[3], o[2]), pack2(o[1], o[0])};
      }
    }
  }
  WAIT_L(0);
#pragma unroll
  for (int e = 0; e < 16; ++e) {
    const int c = e * 8 + (lane >> 3);
    const u32x4 v = *(LAS u32x4*)(wl + c * 144 + (lane & 7) * 16);
    st16(rws, WS_KVT + ch_off(cb * 128 + c) + tblk * 128 + (lane & 7) * 16, v);
  }
  WAIT_V(0);
  flag_set(ws, WS_FLAG + (unsigned)tile_of_block(cb * 32 + (lane & 31)) * FL_STRIDE + FL_KV + tblk * 8, tag);
  const rsrc_t rd = mk_rsrc(a.wd, F_ * D_ * 2), rk = mk_rsrc(a.ssmk, D_ * S_ * 2);
#pragma unroll
  for (int i = 0; i < 2; ++i) blds4(rk, lds + L_PFS, tid * 128, cu * 131072 + i * 65536);
#pragma unroll
  for (int i = 0; i < 4; ++i) blds4(rd, lds + L_PFS, tid * 128, (i * 32 + cu) * 57344);
  WAIT_V(0);
  __syncthreads();
}

DEV u32x2 geglu4(f32x4 gt, f32x4 up) {
  const float C1 = -2.302208198f, C2 = -2.302208198f * 0.044715f;
  const f32x4 wv = gt * (gt * gt * C2 + C1);
  f32x4 ev;
#pragma unroll
  for (int r = 0; r < 4; ++r) ev[r] = __builtin_amdgcn_exp2f(wv[r]);
  const f32x4 dv = ev + 1.0f;
  f32x4 rv;
#pragma unroll
  for (int r = 0; r < 4; ++r) rv[r] = __builtin_amdgcn_rcpf(dv[r]);
  const f32x4 hv = gt * up * rv;
  return (u32x2){pack2(hv[0], hv[1]), pack2(hv[2], hv[3])};
}
DEV void gemm1_epi(const f32x4 (&acc)[4][8], rsrc_t rws, int pm, int pn) {
  const int lane = threadIdx.x & 63;
  const int wid = __builtin_amdgcn_readfirstlane(threadIdx.x >> 6);
  const int wr = wid >> 1, wc = wid & 1;
  const unsigned off = WS_HF + (unsigned)(pn * 4 + wc * 2) * HF_KS + (unsigned)(pm * 16 + wr * 4) * 1024u + lane * 16;
#pragma unroll
  for (int i = 0; i < 4; ++i)
#pragma unroll
    for (int k2 = 0; k2 < 2; ++k2) {
      const u32x2 h0 = geglu4(acc[i][k2 * 2], acc[i][4 + k2 * 2]), h1 = geglu4(acc[i][k2 * 2 + 1], acc[i][4 + k2 * 2 + 1]);
      st16(rws, off + k2 * HF_KS + i * 1024, (u32x4){h0[0], h0[1], h1[0], h1[1]});
    }
}

constexpr int CV_CS = 4160, CV_U = 4 * CV_CS  , CV_SIZE = 23552;
struct ConvIn { u32x4 own[4]; u32x2 nxt[4], h0; u32x4 u[4]; };

DEV void conv_write_seg(LAS char* wl, int seg, u32x4 a, u32x2 b) {
  const unsigned a01 = __builtin_amdgcn_alignbit(a[1], a[0], 16), a12 = __builtin_amdgcn_alignbit(a[2], a[1], 16),
                 a23 = __builtin_amdgcn_alignbit(a[3], a[2], 16), a34 = __builtin_amdgcn_alignbit(b[0], a[3], 16),
                 a45 = __builtin_amdgcn_alignbit(b[1], b[0], 16);
  LAS char* dst = wl + seg * 16;
  *(LAS u32x4*)(dst) = a;
  *(LAS u32x4*)(dst + CV_CS) = (u32x4){a01, a12, a23, a34};
  *(LAS u32x4*)(dst + 2 * CV_CS) = (u32x4){a[1], a[2], a[3], b[0]};
  *(LAS u32x4*)(dst + 3 * CV_CS) = (u32x4){a12, a23, a34, a45};
}
DEV s16x8 conv_ldp(LAS char* p) {
  volatile LAS s16x4* pp = (volatile LAS s16x4*)p;
  const s16x4 lo = pp[0], hi = pp[1];
  return __builtin_shufflevector(lo, hi, 0, 1, 2, 3, 4, 5, 6, 7);
}

template <class Mid>
DEV void conv_wave(LAS char* wl, const ConvIn& in, float dv, rsrc_t rws, int ch, Mid&& mid) {
  const int lane = threadIdx.x & 63;
  const int fr = lane & 15, fq = lane >> 4;
  const u32x4 zero4 = (u32x4){0u, 0u, 0u, 0u};
#pragma unroll
  for (int p = 0; p < 4; ++p) conv_write_seg(wl, 64 * p + lane + 2, in.own[p], in.nxt[p]);
  conv_write_seg(wl, lane & 1, zero4, (lane & 1) ? in.h0 : (u32x2){0u, 0u});
  *(LAS u32x4*)(wl + CV_U + (lane & 31) * 16) = zero4;
#pragma unroll
  for (int i = 0; i < 4; ++i) *(LAS u32x4*)(wl + CV_U + 512 + i * 1024 + lane * 16) = in.u[i];
  const int o_ = 1 + fr + 8 * fq;
  LAS char* pb = wl + (o_ & 3) * CV_CS + 2 * (o_ & ~3);
  LAS char* qb = wl + CV_U + 16 * (33 + 2 * fr - fq);
  f32x4 acc[8];
#pragma unroll
  for (int g = 0; g < 8; ++g) acc[g] = (f32x4){0.f, 0.f, 0.f, 0.f};
  s16x8 P0[8], Q0[8], P1[8], Q1[8];
  WAIT_L(0); SCHED();
#pragma unroll
  for (int a = 0; a < 8; ++a) { P0[a] = conv_ldp(pb + a * 512); Q0[a] = *(LAS s16x8*)(qb + a * 512); }
  SCHED();
  auto cls = [&](s16x8 (&P)[8], s16x8 (&Q)[8], s16x8 (&Pn)[8], s16x8 (&Qn)[8], LAS char* pn, LAS char* qn) AI {
#pragma unroll
    for (int b = 0; b < 8; ++b) {
      Pn[b] = conv_ldp(pn + b * 512); Qn[b] = *(LAS s16x8*)(qn + b * 512);
#pragma unroll
      for (int a = 0; a + b < 8; ++a) mfma16a(acc[a + b], P[a], Q[b]);
      SCHED();
    }
  };
  for (int r = 0; r < 8; r += 2) {
    if (r == 4) mid();
    cls(P0, Q0, P1, Q1, pb + (r + 1) * 64, qb - (r + 1) * 64);
    const int rn = (r + 2) & 7;
    cls(P1, Q1, P0, Q0, pb + rn * 64, qb - rn * 64);
  }
  MFMA_DRAIN();
#pragma unroll
  for (int e = 0; e < 4; ++e) {
    unsigned zx[2][2];
#pragma unroll
    for (int h = 0; h < 2; ++h) {
      const int g = 2 * e + h;
      const int al = 32 * g + 2 * fr + (fq >> 1);
      const u32x2 kvp = *(LAS u32x2*)(wl + CV_U + 512 + al * 16 + ((fq & 1) ? 0 : 8));
      const float k3 = bflo(kvp[0]), k2 = bfhi(kvp[0]), k1 = bflo(kvp[1]), k0 = bfhi(kvp[1]);
      const float z0 = rbf(acc[g][0]) + rbf(k0 * dv), z1 = rbf(acc[g][1]) + rbf(k1 * dv), z2 = rbf(acc[g][2]) + rbf(k2 * dv), z3 = rbf(acc[g][3]) + rbf(k3 * dv);
      zx[h][0] = pack2(z0, z1); zx[h][1] = pack2(z2, z3);
    }
    const auto r0 = __builtin_amdgcn_permlane16_swap(zx[0][0], zx[1][0], false, false);
    const auto r1 = __builtin_amdgcn_permlane16_swap(zx[0][1], zx[1][1], false, false);
    st16(rws, WS_ZT + ch_off(ch) + (unsigned)(256 * (2 * e + (fq & 1)) + 16 * fr + 8 * (fq >> 1)) * 2u, (u32x4){r0[0], r1[0], r0[1], r1[1]});
  }
}

constexpr int L_STG = 8192, L_ZS = L_STG + 9216, L_RING = 4 * CV_SIZE;
static_assert(L_ZS + 5120 <= CV_SIZE && CV_U + 4608 <= CV_SIZE && L_RING + 65536 <= L_PFS, "lds map");

__global__ __launch_bounds__(512, 2) void k1_kernel(const u16* __restrict__ q, const u16* __restrict__ kk, const u16* __restrict__ vv,
                                                    const u16* __restrict__ shk, const u16* __restrict__ ssmk, const u16* __restrict__ ssmD,
                                                    const u16* __restrict__ wg, const u16* __restrict__ wu, const u16* __restrict__ wd,
                                                    const u16* __restrict__ gw, char* ws, u16* __restrict__ out) {
  extern __shared__ __attribute__((aligned(16))) char shm[];
  LAS char* lds = (LAS char*)shm;
  const int tid = threadIdx.x, lane = tid & 63;
  const int wid = __builtin_amdgcn_readfirstlane(tid >> 6);
  const int fr = lane & 15, fq = lane >> 4;
  const int b = blockIdx.x, xcd = b & 7, j = b >> 3;
  const u64 tag = ((hw_dispatch_id() * 0x9E3779B97F4A7C15ull) ^ 0xD1B54A32D192ED03ull) | 1ull;
  const rsrc_t rws = mk_rsrc(ws, WS_BYTES);
  const int rt = (xcd >> 1) * 4 + (j >> 3), ct = (xcd & 1) * 8 + (j & 7);
  const unsigned myfl = WS_FLAG + (unsigned)(rt * 16 + ct) * FL_STRIDE;
  const int ch = b * 4 + (wid & 3);
  const bool light = j < 4;
  const int jj = light ? 0 : j - 4;
  const int pm = (xcd & 1) * 4 + (jj & 3), pn = (xcd >> 1) * 7 + (jj >> 2);
  const Args a{q, kk, vv, shk, ssmk, ssmD, wg, wu, wd, gw, ws, out};

  if (tid < 16) *(volatile LAS unsigned*)(lds + L_CTRL + tid * 4) = (light && tid < 8) ? 1u : 0u;
  ConvIn in;
  const u16* hrow = ssmk + (size_t)ch * S_;
  unsigned dvraw = 0;
  auto conv_issue_h = [&]() AI {
#pragma unroll
    for (int p = 0; p < 4; ++p) {
      in.own[p] = *(const u32x4*)(hrow + 8 * (64 * p + lane));
      const int nx = 64 * p + lane + 1;
      in.nxt[p] = *(const u32x2*)(hrow + 8 * (nx < 256 ? nx : 255));
    }
    in.h0 = *(const u32x2*)hrow;
    dvraw = ssmD[ch];
  };
  auto conv_issue_u = [&]() AI {
#pragma unroll
    for (int i = 0; i < 4; ++i) in.u[i] = ld16(rws, lane * 16 + i * 1024, WS_KVT + ch_off(ch));
  };
  auto drain_in = [&]() AI {
    asm volatile("s_waitcnt vmcnt(0)" : "+v"(in.u[0]), "+v"(in.u[1]), "+v"(in.u[2]), "+v"(in.u[3]), "+v"(in.own[0]), "+v"(in.own[1]), "+v"(in.own[2]), "+v"(in.own[3]),
                 "+v"(in.nxt[0]), "+v"(in.nxt[1]), "+v"(in.nxt[2]), "+v"(in.nxt[3]), "+v"(in.h0), "+v"(dvraw) :: "memory");
  };
  u64 kv0 = 0;
  if (light) {
    light_unit(lds, a, rws, tag, xcd * 4 + j);
    conv_issue_h();
    flag_wait<4>(kv0, ws, myfl + FL_KV, 32, tag);
    conv_issue_u();
    drain_in();
  } else {
    f32x4 acc1[4][8];
    gemm1_main(acc1, kv0, myfl + FL_KV, lds, a, pm, pn, jj & 3, jj >> 2);
    flag_wait<4>(kv0, ws, myfl + FL_KV, 32, tag);
    conv_issue_h();
    conv_issue_u();
    SCHED();
    gemm1_epi(acc1, rws, pm, pn);
    if (wid < 4) __builtin_amdgcn_s_setprio(0);
    drain_in();
    flag_set(ws, WS_FLAG + (unsigned)((pm * 2 + (wid >> 2)) * 16 + (lane & 15)) * FL_STRIDE + FL_H + (((wid & 1) * 28 + pn) * 2 + ((wid >> 1) & 1)) * 8, tag);
  }
  lds_wait4(lds + L_CTRL); lds_wait4(lds + L_CTRL + 16);

  if (wid >= 4) {
    const int w4 = wid - 4;
    const unsigned lo16 = lane * 16;
    const unsigned ha = WS_HF + (unsigned)w4 * HF_KS + (unsigned)(rt * 8) * 1024u;
    const rsrc_t rd = mk_rsrc(wd, F_ * D_ * 2);
    unsigned vbs[2];
#pragma unroll
    for (int gp = 0; gp < 2; ++gp)
      vbs[gp] = (unsigned)((lane >> 3) & 3) * 2048u + (((lane & 7) ^ ((((lane >> 4) & 1) | (gp << 1)) << 1)) * 16) + (lane >> 5) * (16 * 2048u);
    LAS char* ring = lds + L_RING + w4 * 16384;
    unsigned bb[4];
    {
      const int q4 = (lane & 15) >> 2, p = lane & 3;
      const int f2 = (q4 >> 1) | ((fq & 1) << 1);
#pragma unroll
      for (int n = 0; n < 4; ++n) bb[n] = (unsigned)(size_t)ring + (8 * fq + q4) * 128 + ((((n ^ f2) << 1) + (p >> 1)) * 16) + (p & 1) * 8;
    }
    s16x8 A[2][8], B[2][4];
    auto ldA = [&](int s, int i) AI { return __builtin_bit_cast(s16x8, ld16(rws, lo16 + (i & 3) * 1024, ha + (unsigned)s * (4 * HF_KS) + (i >> 2) * 4096)); };
    auto dmaB = [&](int s, int slot) AI {
#pragma unroll
      for (int g = 0; g < 4; ++g) blds16(rd, ring + slot * 4096 + g * 1024, vbs[g & 1], (unsigned)((4 * s + w4) * 32 + 4 * g) * 2048u + ct * 128);
    };
    auto ldB = [&](const int slot, int n) AI {
      return slot == 0 ? tr_read8_asm<0, 512>(bb[n]) : slot == 1 ? tr_read8_asm<4096, 512>(bb[n]) : slot == 2 ? tr_read8_asm<8192, 512>(bb[n]) : tr_read8_asm<12288, 512>(bb[n]);
    };
    const unsigned hfo = myfl + FL_H + (w4 >> 1) * 448;
    u64 rdy = 0; int nxt = 0;
    auto poll_eval = [&](u64 v) AI {
      rdy = __ballot(v == tag);
      __builtin_amdgcn_fence(__ATOMIC_ACQUIRE, "wavefront");
    };
    auto need = [&](const int upto) AI {
      const u64 mask = (1ull << (2 * upto)) - 1ull;
      for (unsigned spins = 0; (rdy & mask) != mask && spins < (1u << 16); ++spins) {
        if (spins) __builtin_amdgcn_s_sleep(4);
        poll_eval(flag_peek(ws, hfo, 56));
      }
    };
    auto next = [&]() AI -> int { return nxt++; };
    dmaB(0, 0); dmaB(1, 1); dmaB(2, 2);
    need(11);
    WAIT_V(0);
    __builtin_amdgcn_s_setprio(3);
    const int o0 = next(), o1 = next();
    int q2 = next(), q3;
#pragma unroll
    for (int i = 0; i < 8; ++i) A[0][i] = ldA(o0, i);
#pragma unroll
    for (int i = 0; i < 8; ++i) A[1][i] = ldA(o1, i);
    f32x4 acc[8][4];
#pragma unroll
    for (int n = 0; n < 4; ++n) B[0][n] = ldB(0, n);
    SCHED();
    auto step = [&](const int sq, const bool first, const bool pre3, const bool pre2, const bool pre1) AI {
      const int sl = sq & 1;
      if (pre3) q3 = next();
      WAIT_L(0);
      if (pre3) dmaB(q3, (sq + 3) & 3);
      if (pre1) {
        if (pre3) { WAIT_V(24); } else if (pre2) { WAIT_V(20); } else { WAIT_V(8); }
#pragma unroll
        for (int n = 0; n < 4; ++n) B[sl ^ 1][n] = ldB((sq + 1) & 3, n);
      }
      SCHED();
#pragma unroll
      for (int i = 0; i < 8; ++i) {
#pragma unroll
        for (int n = 0; n < 4; ++n) { if (first) mfma16z(acc[i][n], B[sl][n], A[sl][i]); else mfma16a(acc[i][n], B[sl][n], A[sl][i]); }
        if (pre2) A[sl][i] = ldA(q2, i);
        SCHED();
      }
      q2 = q3;
    };
    step(0, true, true, true, true); step(1, false, true, true, true); step(2, false, true, true, true); step(3, false, true, true, true);
    u64 pv = flag_peek(ws, hfo, 56);
    for (int s = 4; s < 24; s += 4) {
      if (s == 8) poll_eval(pv);
      if (s == 20) *(volatile LAS unsigned*)(lds + L_CTRL + 48 + w4 * 4) = 1u;
      need(s + 7);
      step(0, false, true, true, true); step(1, false, true, true, true); step(2, false, true, true, true); step(3, false, true, true, true);
    }
    need(28);
    step(0, false, true, true, true); step(1, false, false, true, true); step(2, false, false, false, true); step(3, false, false, false, false);
    MFMA_DRAIN();
    lds_wait4(lds + L_CTRL + 32);
#pragma unroll
    for (int o = 0; o < 4; ++o) {
      if (o != w4) {
        const int d = (o < w4) ? o : o - 1;
        LAS char* dst = (d < 2) ? ring + d * 8192 : lds + w4 * CV_SIZE;
#pragma unroll
        for (int i2 = 0; i2 < 2; ++i2)
#pragma unroll
          for (int n = 0; n < 4; ++n) *(LAS f32x4*)(dst + (i2 * 4 + n) * 1024 + lo16) = acc[o * 2 + i2][n];
      }
    }
    f32x4 fin[2][4];
#pragma unroll
    for (int o = 0; o < 4; ++o)
      if (o == w4) {
#pragma unroll
        for (int i2 = 0; i2 < 2; ++i2)
#pragma unroll
          for (int n = 0; n < 4; ++n) fin[i2][n] = acc[o * 2 + i2][n];
      }
    WAIT_L(0);
    BAR();
    asm volatile("" ::: "memory");
#pragma unroll
    for (int k = 1; k < 4; ++k) {
      const int sw = (w4 + k) & 3;
      const int d = (w4 < sw) ? w4 : w4 - 1;
      LAS char* src = (d < 2) ? lds + L_RING + sw * 16384 + d * 8192 : lds + sw * CV_SIZE;
#pragma unroll
      for (int i2 = 0; i2 < 2; ++i2)
#pragma unroll
        for (int n = 0; n < 4; ++n) fin[i2][n] += *(LAS f32x4*)(src + (i2 * 4 + n) * 1024 + lo16);
    }
    LAS char* sg = lds + w4 * CV_SIZE + L_STG;
#pragma unroll
    for (int i2 = 0; i2 < 2; ++i2) {
      const int row = rt * 128 + (w4 * 2 + i2) * 16 + fr;
      const float og = *(LAS float*)(sg + 8192 + i2 * 256 + lane * 4);
      unsigned ow[4][2];
#pragma unroll
      for (int n = 0; n < 4; ++n) {
        const f32x4 ga = *(LAS f32x4*)(sg + (i2 * 4 + n) * 1024 + lo16);
        float r4[4];
#pragma unroll
        for (int r = 0; r < 4; ++r) r4[r] = ga[r] + rbf(og * rbf(fin[i2][n][r]));
        ow[n][0] = pack2(r4[0], r4[1]); ow[n][1] = pack2(r4[2], r4[3]);
      }
#pragma unroll
      for (int n2 = 0; n2 < 2; ++n2) {
        const auto r0 = __builtin_amdgcn_permlane16_swap(ow[2 * n2][0], ow[2 * n2 + 1][0], false, false);
        const auto r1 = __builtin_amdgcn_permlane16_swap(ow[2 * n2][1], ow[2 * n2 + 1][1], false, false);
        *(u32x4*)(out + (size_t)row * D_ + ct * 64 + n2 * 32 + (fq & 1) * 16 + (fq >> 1) * 8) = (u32x4){r0[0], r1[0], r0[1], r1[1]};
      }
    }
  } else {
    const float dv = bflo(dvraw);
    u32x4 qr[4], gx[2][2], gwv[2];
#pragma unroll
    for (int i = 0; i < 4; ++i) qr[i] = (u32x4){0u, 0u, 0u, 0u};
    gx[0][0] = gx[0][1] = gx[1][0] = gx[1][1] = gwv[0] = gwv[1] = (u32x4){0u, 0u, 0u, 0u};
    auto side_loads = [&]() AI {
#pragma unroll
    for (int i = 0; i < 4; ++i) qr[i] = *(const u32x4*)(q + (size_t)(rt * 128 + wid * 32 + i * 8 + (lane >> 3)) * D_ + ct * 64 + (lane & 7) * 8);
#pragma unroll
    for (int r = 0; r < 2; ++r) {
      const u16* xr = q + (size_t)(b * 8 + wid * 2 + r) * D_ + lane * 8;
      gx[r][0] = *(const u32x4*)xr; gx[r][1] = *(const u32x4*)(xr + 512);
    }
    gwv[0] = *(const u32x4*)(gw + lane * 8); gwv[1] = *(const u32x4*)(gw + 512 + lane * 8);
    };
    SCHED();
    conv_wave(lds + wid * CV_SIZE, in, dv, rws, ch, side_loads);
    {
      float sg2[2];
#pragma unroll
      for (int r = 0; r < 2; ++r) {
        float s = 0.f;
#pragma unroll
        for (int e = 0; e < 4; ++e) {
          s += rbf(bflo(gx[r][0][e]) * bflo(gwv[0][e])) + rbf(bfhi(gx[r][0][e]) * bfhi(gwv[0][e]));
          s += rbf(bflo(gx[r][1][e]) * bflo(gwv[1][e])) + rbf(bfhi(gx[r][1][e]) * bfhi(gwv[1][e]));
        }
#pragma unroll
        for (int o = 32; o >= 1; o >>= 1) s += __shfl_xor(s, o);
        sg2[r] = s;
      }
      const float s = rbf((lane & 1) ? sg2[1] : sg2[0]);
      const float g = rbf(1.0f / (1.0f + __expf(-s)));
      __builtin_amdgcn_raw_buffer_store_b32(__float_as_uint(g), rws, WS_G + (b * 8 + wid * 2 + (lane & 1)) * 4, 0, SC1);
    }
    WAIT_V(0);
    flag_set(ws, WS_FLAG + ((lane & 16) ? (unsigned)((b >> 4) * 16 + (lane & 15)) * FL_STRIDE + FL_G + ((b & 15) * 4 + wid) * 8
                                        : (unsigned)((lane & 15) * 16 + (ch >> 6)) * FL_STRIDE + FL_Z + (ch & 63) * 8), tag);
    *(volatile LAS unsigned*)(lds + L_CTRL + 32 + wid * 4) = 1u;
    LAS char* sg = lds + wid * CV_SIZE + L_STG;
    LAS char* zs = lds + wid * CV_SIZE + L_ZS;
#pragma unroll
    for (int i = 0; i < 4; ++i) *(LAS u32x4*)(sg + (i * 8 + (lane >> 3)) * 144 + (lane & 7) * 16) = qr[i];
    WAIT_L(0);
    u32x2 qv[2][4];
#pragma unroll
    for (int i2 = 0; i2 < 2; ++i2)
#pragma unroll
      for (int n = 0; n < 4; ++n) qv[i2][n] = *(LAS u32x2*)(sg + (i2 * 16 + fr) * 144 + n * 32 + fq * 8);
    lds_wait4<16>(lds + L_CTRL + 48);
    {
      const unsigned tl = (unsigned)tag, th = (unsigned)(tag >> 32);
      for (unsigned spins = 0; spins < (1u << 16); ++spins) {
        const u32x4 v = ld16(rws, lane * 16, myfl + FL_G);
        if (__all(v[0] == tl && v[1] == th && v[2] == tl && v[3] == th)) break;
        __builtin_amdgcn_s_sleep(16);
      }
      asm volatile("" ::: "memory");
      __builtin_amdgcn_fence(__ATOMIC_ACQUIRE, "wavefront");
    }
    float gv[2];
    {
      u32x4 zr[4];
#pragma unroll
      for (int i2 = 0; i2 < 2; ++i2) gv[i2] = __uint_as_float(__builtin_amdgcn_raw_buffer_load_b32(rws, (rt * 128 + (wid * 2 + i2) * 16 + fr) * 4, WS_G, SC1));
#pragma unroll
      for (int i = 0; i < 4; ++i) zr[i] = ld16(rws, (unsigned)(ct * 65 + i * 16 + (lane >> 2)) * 4096u + (lane & 3) * 16, WS_ZT + (unsigned)(rt * 128 + wid * 32) * 2u);
#pragma unroll
      for (int i = 0; i < 4; ++i) *(LAS u32x4*)(zs + (i * 16 + (lane >> 2)) * 80 + (lane & 3) * 16) = zr[i];
    }
    WAIT_L(0);
    const int q4 = (lane & 15) >> 2, p = lane & 3;
#pragma unroll
    for (int i2 = 0; i2 < 2; ++i2) {
      const float g = gv[i2];
#pragma unroll
      for (int n = 0; n < 4; ++n) {
        const s16x4 zz = __builtin_amdgcn_ds_read_tr16_b64_v4i16((LAS s16x4*)(zs + (16 * n + 4 * fq + q4) * 80 + (i2 * 16 + 4 * p) * 2));
        const u32x2 zu = __builtin_bit_cast(u32x2, zz);
        const unsigned q01 = qv[i2][n][0], q23 = qv[i2][n][1];
        f32x4 ga;
        ga[0] = rbf(g * rbf(bflo(q01) * bflo(zu[0]))); ga[1] = rbf(g * rbf(bfhi(q01) * bfhi(zu[0])));
        ga[2] = rbf(g * rbf(bflo(q23) * bflo(zu[1]))); ga[3] = rbf(g * rbf(bfhi(q23) * bfhi(zu[1])));
        *(LAS f32x4*)(sg + (i2 * 4 + n) * 1024 + lane * 16) = ga;
      }
      *(LAS float*)(sg + 8192 + i2 * 256 + lane * 4) = rbf(1.0f - g);
    }
    WAIT_L(0);
    BAR();
    if (wid < 3) *(u32x4*)(ws + myfl + (wid * 64 + lane) * 16) = (u32x4){0u, 0u, 0u, 0u};
  }
}

extern "C" void kernel_launch(void* const* d_in, const int* in_sizes, int n_in, void* d_out, int out_size, void* d_ws, size_t ws_size,
                              hipStream_t stream) {
  const u16* q = (const u16*)d_in[0]; const u16* kk = (const u16*)d_in[1]; const u16* vv = (const u16*)d_in[2];
  const u16* shk = (const u16*)d_in[3]; const u16* ssmk = (const u16*)d_in[4]; const u16* ssmD = (const u16*)d_in[5];
  const u16* wg = (const u16*)d_in[7]; const u16* wu = (const u16*)d_in[8]; const u16* wd = (const u16*)d_in[9];
  const u16* gw = (const u16*)d_in[10];
  constexpr int LDS1 = 163840;
  (void)hipFuncSetAttribute((const void*)k1_kernel, hipFuncAttributeMaxDynamicSharedMemorySize, LDS1);
  k1_kernel<<<256, 512, LDS1, stream>>>(q, kk, vv, shk, ssmk, ssmD, wg, wu, wd, gw, (char*)d_ws, (u16*)d_out);
}
```

```cpp
#include <hip/hip_runtime.h>
#include <hip/hip_bf16.h>

typedef unsigned short u16;
typedef unsigned long long u64;
typedef short s16x4 __attribute__((ext_vector_type(4)));
typedef short s16x8 __attribute__((ext_vector_type(8)));
typedef __bf16 bfx2 __attribute__((ext_vector_type(2)));
typedef float f32x4 __attribute__((ext_vector_type(4)));
typedef unsigned u32x4 __attribute__((ext_vector_type(4)));
typedef unsigned u32x2 __attribute__((ext_vector_type(2)));
typedef __amdgpu_buffer_rsrc_t rsrc_t;
#define LAS __attribute__((address_space(3)))
#define GAS __attribute__((address_space(1)))
#define DEV __device__ __forceinline__
#define AI __attribute__((always_inline))
extern "C" __device__ u64 hw_dispatch_id() __asm("llvm.amdgcn.dispatch.id");

constexpr int S_ = 2048, D_ = 1024, F_ = 3584;
constexpr unsigned WS_HF = 0;
constexpr unsigned HF_KS = 129 * 1024;
constexpr unsigned WS_KVT = 16u << 20;
constexpr unsigned WS_ZT = 21u << 20;
constexpr unsigned WS_G = 26u << 20;
constexpr unsigned WS_FLAG = 27u << 20;
constexpr unsigned WS_BYTES = 28u << 20;
__device__ constexpr unsigned ch_off(unsigned ch) { return (ch + (ch >> 6)) * 4096u; }
constexpr int FL_H = 0, FL_G = 1024, FL_Z = 1536, FL_KV = 2048;
constexpr unsigned FL_STRIDE = 4096;
constexpr int SC1 = 16;
constexpr int L_PFS = 163840 - 512, L_CTRL = 163840 - 64;

DEV float bflo(unsigned w) { return __uint_as_float(w << 16); }
DEV float bfhi(unsigned w) { return __uint_as_float(w & 0xffff0000u); }
DEV unsigned pack2(float lo, float hi) { bfx2 p; p[0] = (__bf16)lo; p[1] = (__bf16)hi; return __builtin_bit_cast(unsigned, p); }
DEV float rbf(float f) { return (float)(__bf16)f; }
DEV void mfma16a(f32x4& c, s16x8 a, s16x8 b) {
  asm volatile("v_mfma_f32_16x16x32_bf16 %0, %1, %2, %0" : "+v"(c) : "v"(a), "v"(b));
}
DEV void mfma16z(f32x4& c, s16x8 a, s16x8 b) {
  asm volatile("v_mfma_f32_16x16x32_bf16 %0, %1, %2, 0" : "=&v"(c) : "v"(a), "v"(b));
}
template <int OFF, int D2>
DEV s16x8 tr_read8_asm(unsigned addr) {
  s16x4 lo, hi;
  asm volatile("ds_read_b64_tr_b16 %0, %2 offset:%3\n\tds_read_b64_tr_b16 %1, %2 offset:%4" : "=&v"(lo), "=&v"(hi) : "v"(addr), "i"(OFF), "i"(OFF + D2));
  return __builtin_shufflevector(lo, hi, 0, 1, 2, 3, 4, 5, 6, 7);
}
template <int OFF>
DEV s16x8 ds_read16_asm(unsigned addr) {
  s16x8 r;
  asm volatile("ds_read_b128 %0, %1 offset:%2" : "=v"(r) : "v"(addr), "i"(OFF));
  return r;
}
template <int V> struct IC { static constexpr int value = V; };
DEV rsrc_t mk_rsrc(const void* p, unsigned bytes) { return __builtin_amdgcn_make_buffer_rsrc((void*)p, 0, bytes, 0x00020000); }
DEV void blds16(rsrc_t r, LAS char* l, unsigned voff, unsigned soff) {
  __builtin_amdgcn_raw_ptr_buffer_load_lds(r, (LAS void*)l, 16, voff, soff, 0, 0);
}
DEV void blds4(rsrc_t r, LAS char* l, unsigned voff, unsigned soff) {
  __builtin_amdgcn_raw_ptr_buffer_load_lds(r, (LAS void*)l, 4, voff, soff, 0, 0);
}
DEV u32x4 ld16(rsrc_t r, unsigned voff, unsigned soff) { return __builtin_amdgcn_raw_buffer_load_b128(r, voff, soff, SC1); }
DEV void st16(rsrc_t r, unsigned voff, u32x4 v) { __builtin_amdgcn_raw_buffer_store_b128(v, r, voff, 0, SC1); }
#define WAIT_V(n) asm volatile("s_waitcnt vmcnt(" #n ")" ::: "memory")
#define WAIT_L(n) asm volatile("s_waitcnt lgkmcnt(" #n ")" ::: "memory")
#define BAR() __builtin_amdgcn_s_barrier()
#define SCHED() __builtin_amdgcn_sched_barrier(0)
#define MFMA_DRAIN() do { asm volatile("s_nop 7\n\ts_nop 7" ::: "memory"); SCHED(); } while (0)

DEV void flag_set(char* ws, unsigned off, u64 tag) { __hip_atomic_store((GAS u64*)(ws + off), tag, __ATOMIC_RELAXED, __HIP_MEMORY_SCOPE_AGENT); }
DEV u64 flag_peek(char* ws, unsigned off, int n) {
  const int lane = threadIdx.x & 63;
  return __hip_atomic_load((GAS u64*)(ws + off + 8 * (lane < n ? lane : 0)), __ATOMIC_RELAXED, __HIP_MEMORY_SCOPE_AGENT);
}
template <int SLP>
DEV void flag_wait(u64 first, char* ws, unsigned off, int n, u64 tag) {
  u64 v = first;
  for (unsigned spins = 0; spins < (1u << 16); ++spins) {
    if (__all(v == tag)) break;
    __builtin_amdgcn_s_sleep(SLP);
    v = flag_peek(ws, off, n);
  }
  __builtin_amdgcn_fence(__ATOMIC_ACQUIRE, "wavefront");
}
template <int SLP = 1>
DEV void lds_wait4(LAS char* p) {
  for (unsigned spins = 0; spins < (1u << 22); ++spins) {
    const u32x4 dn = *(volatile LAS u32x4*)p;
    if (__builtin_amdgcn_readfirstlane(dn[0] & dn[1] & dn[2] & dn[3]) != 0u) break;
    __builtin_amdgcn_s_sleep(SLP);
  }
  asm volatile("" ::: "memory");
}
DEV int tile_of_block(int b) { const int xcd = b & 7, j = b >> 3; return ((xcd >> 1) * 4 + (j >> 3)) * 16 + (xcd & 1) * 8 + (j & 7); }

struct Args {
  const u16 *q, *kk, *vv, *shk, *ssmk, *ssmD, *wg, *wu, *wd, *gw;
  char* ws; u16* out;
};

DEV void gemm1_main(f32x4 (&acc)[4][8], u64& kvpeek, unsigned kvoff, LAS char* lds, const Args& a, int pm, int pn, int im, int in_) {
  char* ws = a.ws;
  const int tid = threadIdx.x, lane = tid & 63;
  const int wid = __builtin_amdgcn_readfirstlane(tid >> 6);
  const int wr = wid >> 1, wc = wid & 1;
  const int fr = lane & 15, fq = lane >> 4;
  unsigned va[2], vb[2];
#pragma unroll
  for (int e = 0; e < 2; ++e) {
    const int rl = lane >> 3, p = lane & 7;
    const int sw = (e << 2) | (lane >> 4);
    va[e] = (unsigned)(rl * 2048 + ((p ^ sw) * 16));
    const int kl = lane >> 4, pb = lane & 15;
    const int swb = (kl | (e << 2)) << 1;
    vb[e] = (unsigned)(kl * (F_ * 2) + (pb ^ swb) * 16);
  }
  const rsrc_t rx = mk_rsrc(a.q, S_ * D_ * 2);
  const rsrc_t rw = mk_rsrc((wid < 4) ? a.wg : a.wu, D_ * F_ * 2);
  const unsigned xa_base = (unsigned)(pm * 256 + wid * 32) * 2048u;
  const unsigned wb_base = (unsigned)((wid & 3) * 16) * (F_ * 2) + pn * 256;
  LAS char* ldsA = lds + wid * 4096;
  LAS char* ldsB = lds + 65536 + wid * 4096;
  auto dma = [&](int kt, int st, int d) AI {
    const int i = d >> 1;
    if (d & 1) blds16(rw, ldsB + st * 32768 + i * 1024, vb[i >> 1], wb_base + (kt * 64 + i * 4) * (F_ * 2));
    else blds16(rx, ldsA + st * 32768 + i * 1024, va[i & 1], xa_base + i * (8 * 2048) + kt * 128);
  };
#pragma unroll
  for (int d = 0; d < 8; ++d) dma(0, 0, d);
  SCHED(); BAR(); SCHED();
#pragma unroll
  for (int d = 0; d < 8; ++d) dma(1, 1, d);
  SCHED();
  rsrc_t rp; unsigned vp, pstep;
  if (wid < 4) {
    rp = mk_rsrc((im >> 1) ? a.wu : a.wg, D_ * F_ * 2);
    vp = (unsigned)(wid * 16 + (lane & 15)) * (F_ * 2) + pn * 256 + (im & 1) * 128; pstep = 64 * F_ * 2;
  } else {
    rp = rx;
    int r = in_ * 37 + (wid - 4) * 10 + ((lane & 15) < 10 ? (lane & 15) : 9); r = r > 255 ? 255 : r;
    vp = (unsigned)(pm * 256 + r) * 2048u; pstep = 128;
  }
  vp += (unsigned)(lane >> 4) * 32u;
  auto pf = [&](int kt) AI { blds4(rp, lds + L_PFS, vp, kt * pstep); };
  const unsigned lds0 = (unsigned)(size_t)lds;
  unsigned bx[2];
#pragma unroll
  for (int ks = 0; ks < 2; ++ks) bx[ks] = lds0 + (wr * 64 + fr) * 128 + (((ks * 4 + fq) ^ (fr >> 1)) * 16);
  unsigned bw[4];
  {
    const int q = (lane & 15) >> 2, p = lane & 3;
    const int sw = (q | ((fq & 1) << 2)) << 1;
#pragma unroll
    for (int j = 0; j < 4; ++j) bw[j] = lds0 + 65536 + (8 * fq + q) * 256 + (((wc * 8 + j * 2 + (p >> 1)) ^ sw) * 16) + (p & 1) * 8;
  }
  auto ldx = [&](auto ST, auto KS, const int i) AI -> s16x8 {
    constexpr int o = decltype(ST)::value * 32768; const unsigned p = bx[decltype(KS)::value];
    return i == 0 ? ds_read16_asm<o>(p) : i == 1 ? ds_read16_asm<o + 2048>(p) : i == 2 ? ds_read16_asm<o + 4096>(p) : ds_read16_asm<o + 6144>(p);
  };
  auto ldw = [&](auto ST, auto KS, const int n) AI -> s16x8 {
    constexpr int o = decltype(ST)::value * 32768 + decltype(KS)::value * 8192;
    return n < 4 ? tr_read8_asm<o, 1024>(bw[n & 3]) : tr_read8_asm<o + 16384, 1024>(bw[n & 3]);
  };
  s16x8 xa[4], xb[4], w[8];
  pf(2);
  WAIT_V(9); BAR(); SCHED();
#pragma unroll
  for (int i = 0; i < 4; ++i) xa[i] = ldx(IC<0>{}, IC<0>{}, i);
#pragma unroll
  for (int n = 0; n < 8; ++n) w[n] = ldw(IC<0>{}, IC<0>{}, n);
  pf(3);
  WAIT_L(0);
  SCHED();

  auto tile_body = [&](auto ST, const int kt, const bool pre1, const bool pre2, const int hook) AI {
    constexpr int st = decltype(ST)::value;
#pragma unroll
    for (int n = 0; n < 8; ++n) {
      if (pre2) { if (n == 2) __builtin_amdgcn_s_setprio(1); if (n == 6) __builtin_amdgcn_s_setprio(0); }
      if (n == 6) WAIT_L(15);
#pragma unroll
      for (int i = 0; i < 4; ++i) {
        if (n == 0) { if (i == 0) WAIT_L(13); if (i == 1) WAIT_L(10); if (i == 2) WAIT_L(7); if (i == 3) WAIT_L(4); }
        if (hook == 1) mfma16z(acc[i][n], w[n], xa[i]); else mfma16a(acc[i][n], w[n], xa[i]);
      }
      w[n] = ldw(ST, IC<1>{}, n);
      if (n < 4) xb[n] = ldx(ST, IC<1>{}, n);
      if (n == 3 && hook <= 1) pf(kt + 4);
      if (n == 3 && hook == 5) kvpeek = flag_peek(ws, kvoff, 32);
      SCHED();
    }
#pragma unroll
    for (int n = 0; n < 2; ++n) {
#pragma unroll
      for (int i = 0; i < 4; ++i) {
        if (n == 0) { if (i == 0) WAIT_L(15); if (i == 1) WAIT_L(14); if (i == 2) WAIT_L(11); if (i == 3) WAIT_L(8); }
        mfma16a(acc[i][n], w[n], xb[i]);
      }
    }
    SCHED();
    WAIT_L(0);
    if (pre1) {
      if (hook == 1) { WAIT_V(3); } else if (hook == 7) { WAIT_V(0); } else { WAIT_V(1); }
      BAR(); SCHED();
      if (pre2) __builtin_amdgcn_s_setprio(3);
      else if (wid < 4) __builtin_amdgcn_s_setprio(2);
      else __builtin_amdgcn_s_setprio(1);
      w[0] = ldw(IC<(st ^ 1)>{}, IC<0>{}, 0); w[1] = ldw(IC<(st ^ 1)>{}, IC<0>{}, 1);
      if (pre2) { dma(kt + 2, st, 0); dma(kt + 2, st, 1); }
      SCHED();
    }
#pragma unroll
    for (int n = 2; n < 8; ++n) {
      if (pre2 && n == 6) __builtin_amdgcn_s_setprio(2);
#pragma unroll
      for (int i = 0; i < 4; ++i) mfma16a(acc[i][n], w[n], xb[i]);
      if (pre1) {
        w[n] = ldw(IC<(st ^ 1)>{}, IC<0>{}, n);
        if (n - 2 < 4) xa[n - 2] = ldx(IC<(st ^ 1)>{}, IC<0>{}, n - 2);
        if (pre2) dma(kt + 2, st, n);
      }
      SCHED();
    }
  };
  tile_body(IC<0>{}, 0, true, true, 1);
  tile_body(IC<1>{}, 1, true, true, 0);
  for (int kt = 2; kt < 12; kt += 2) {
    tile_body(IC<0>{}, kt, true, true, 0);
    tile_body(IC<1>{}, kt + 1, true, true, 0);
  }
  tile_body(IC<0>{}, 12, true, true, 7);
  tile_body(IC<1>{}, 13, true, true, 7);
  tile_body(IC<0>{}, 14, true, false, 5);
  tile_body(IC<1>{}, 15, false, false, 7);
  *(volatile LAS unsigned*)(lds + L_CTRL + wid * 4) = 1u;
  MFMA_DRAIN();
}

DEV void light_unit(LAS char* lds, const Args& a, rsrc_t rws, u64 tag, int cu) {
  char* ws = a.ws;
  const int tid = threadIdx.x, lane = tid & 63;
  const int wid = __builtin_amdgcn_readfirstlane(tid >> 6);
  const int cb = cu & 7, tblk = (cu >> 3) * 8 + wid;
  const int kch = cb * 128 + lane * 2;
  LAS char* wl = lds + wid * (128 * 144);
  {
    const int hj = cu & 3;
    if (wid == 7) {
      if (hj == 0) { blds4(mk_rsrc(a.wg, D_ * F_ * 2), lds + L_PFS, lane * 65536, 0); blds4(mk_rsrc(a.wg, D_ * F_ * 2), lds + L_PFS, lane * 65536, 64 * 65536); blds4(mk_rsrc(a.q, S_ * D_ * 2), lds + L_PFS, lane * 65536, 0); }
      if (hj == 1) { blds4(mk_rsrc(a.wu, D_ * F_ * 2), lds + L_PFS, lane * 65536, 0); blds4(mk_rsrc(a.wu, D_ * F_ * 2), lds + L_PFS, lane * 65536, 64 * 65536); blds4(mk_rsrc(a.out, S_ * D_ * 2), lds + L_PFS, lane * 65536, 0); }
      if (hj == 2) { blds4(mk_rsrc(a.wd, D_ * F_ * 2), lds + L_PFS, lane * 65536, 0); blds4(mk_rsrc(a.wd, D_ * F_ * 2), lds + L_PFS, lane * 65536, 64 * 65536); blds4(mk_rsrc(a.ssmk, S_ * D_ * 2), lds + L_PFS, lane * 65536, 0); }
      if (hj == 3) {
        blds4(rws, lds + L_PFS, lane * HF_KS + 131072, WS_HF); blds4(rws, lds + L_PFS, (lane < 48 ? lane : 47) * HF_KS + 131072, WS_HF + 64 * HF_KS);
        blds4(rws, lds + L_PFS, (lane & 15) * (65 * 4096u) + 64 * 4096u + ((lane & 16) ? WS_ZT : WS_KVT), 0);
        blds4(rws, lds + L_PFS, (lane & 15) * 65536 + 4032, WS_FLAG);
      }
    }
  }
  const u32x4 shw = *(const u32x4*)(a.shk + kch * 4);
#pragma unroll
  for (int hf = 0; hf < 2; ++hf) {
    const int t0 = tblk * 64 + hf * 32;
    unsigned kr[35], vr[32];
#pragma unroll
    for (int jx = 0; jx < 35; ++jx) { const int t = t0 - 3 + jx; kr[jx] = *(const unsigned*)(a.kk + (size_t)(t < 0 ? 0 : t) * D_ + kch); }
#pragma unroll
    for (int jx = 0; jx < 32; ++jx) vr[jx] = *(const unsigned*)(a.vv + (size_t)(t0 + jx) * D_ + kch);
    SCHED();
#pragma unroll
    for (int c = 0; c < 2; ++c) {
      const unsigned s01 = shw[c * 2], s23 = shw[c * 2 + 1];
      const float w0 = bflo(s01), w1 = bfhi(s01), w2 = bflo(s23), w3 = bfhi(s23);
#pragma unroll
      for (int sb = 0; sb < 4; ++sb) {
        float kf[11];
#pragma unroll
        for (int jx = 0; jx < 11; ++jx) { kf[jx] = c ? bfhi(kr[sb * 8 + jx]) : bflo(kr[sb * 8 + jx]); if (sb == 0 && jx < 3 && t0 == 0) kf[jx] = 0.f; }
        float o[8];
#pragma unroll
        for (int jx = 0; jx < 8; ++jx) o[jx] = (kf[jx] * w3 + kf[jx + 1] * w2 + kf[jx + 2] * w1 + kf[jx + 3] * w0) * (c ? bfhi(vr[sb * 8 + jx]) : bflo(vr[sb * 8 + jx]));
        *(LAS u32x4*)(wl + (lane * 2 + c) * 144 + (hf * 4 + sb) * 16) = (u32x4){pack2(o[7], o[6]), pack2(o[5], o[4]), pack2(o[3], o[2]), pack2(o[1], o[0])};
      }
    }
  }
  WAIT_L(0);
#pragma unroll
  for (int e = 0; e < 16; ++e) {
    const int c = e * 8 + (lane >> 3);
    const u32x4 v = *(LAS u32x4*)(wl + c * 144 + (lane & 7) * 16);
    st16(rws, WS_KVT + ch_off(cb * 128 + c) + tblk * 128 + (lane & 7) * 16, v);
  }
  WAIT_V(0);
  flag_set(ws, WS_FLAG + (unsigned)tile_of_block(cb * 32 + (lane & 31)) * FL_STRIDE + FL_KV + tblk * 8, tag);
  const rsrc_t rd = mk_rsrc(a.wd, F_ * D_ * 2), rk = mk_rsrc(a.ssmk, D_ * S_ * 2);
#pragma unroll
  for (int i = 0; i < 2; ++i) blds4(rk, lds + L_PFS, tid * 128, cu * 131072 + i * 65536);
#pragma unroll
  for (int i = 0; i < 4; ++i) blds4(rd, lds + L_PFS, tid * 128, (i * 32 + cu) * 57344);
  WAIT_V(0);
  __syncthreads();
}

DEV u32x2 geglu4(f32x4 gt, f32x4 up) {
  const float C1 = -2.302208198f, C2 = -2.302208198f * 0.044715f;
  const f32x4 wv = gt * (gt * gt * C2 + C1);
  f32x4 ev;
#pragma unroll
  for (int r = 0; r < 4; ++r) ev[r] = __builtin_amdgcn_exp2f(wv[r]);
  const f32x4 dv = ev + 1.0f;
  f32x4 rv;
#pragma unroll
  for (int r = 0; r < 4; ++r) rv[r] = __builtin_amdgcn_rcpf(dv[r]);
  const f32x4 hv = gt * up * rv;
  return (u32x2){pack2(hv[0], hv[1]), pack2(hv[2], hv[3])};
}
DEV void gemm1_epi(const f32x4 (&acc)[4][8], rsrc_t rws, int pm, int pn) {
  const int lane = threadIdx.x & 63;
  const int wid = __builtin_amdgcn_readfirstlane(threadIdx.x >> 6);
  const int wr = wid >> 1, wc = wid & 1;
  const unsigned off = WS_HF + (unsigned)(pn * 4 + wc * 2) * HF_KS + (unsigned)(pm * 16 + wr * 4) * 1024u + lane * 16;
#pragma unroll
  for (int i = 0; i < 4; ++i)
#pragma unroll
    for (int k2 = 0; k2 < 2; ++k2) {
      const u32x2 h0 = geglu4(acc[i][k2 * 2], acc[i][4 + k2 * 2]), h1 = geglu4(acc[i][k2 * 2 + 1], acc[i][4 + k2 * 2 + 1]);
      st16(rws, off + k2 * HF_KS + i * 1024, (u32x4){h0[0], h0[1], h1[0], h1[1]});
    }
}

constexpr int CV_CS = 4160, CV_U = 4 * CV_CS  , CV_SIZE = 23552;
struct ConvIn { u32x4 own[4]; u32x2 nxt[4], h0; u32x4 u[4]; };

DEV void conv_write_seg(LAS char* wl, int seg, u32x4 a, u32x2 b) {
  const unsigned a01 = __builtin_amdgcn_alignbit(a[1], a[0], 16), a12 = __builtin_amdgcn_alignbit(a[2], a[1], 16),
                 a23 = __builtin_amdgcn_alignbit(a[3], a[2], 16), a34 = __builtin_amdgcn_alignbit(b[0], a[3], 16),
                 a45 = __builtin_amdgcn_alignbit(b[1], b[0], 16);
  LAS char* dst = wl + seg * 16;
  *(LAS u32x4*)(dst) = a;
  *(LAS u32x4*)(dst + CV_CS) = (u32x4){a01, a12, a23, a34};
  *(LAS u32x4*)(dst + 2 * CV_CS) = (u32x4){a[1], a[2], a[3], b[0]};
  *(LAS u32x4*)(dst + 3 * CV_CS) = (u32x4){a12, a23, a34, a45};
}
DEV s16x8 conv_ldp(LAS char* p) {
  volatile LAS s16x4* pp = (volatile LAS s16x4*)p;
  const s16x4 lo = pp[0], hi = pp[1];
  return __builtin_shufflevector(lo, hi, 0, 1, 2, 3, 4, 5, 6, 7);
}

template <class Mid>
DEV void conv_wave(LAS char* wl, const ConvIn& in, float dv, rsrc_t rws, int ch, Mid&& mid) {
  const int lane = threadIdx.x & 63;
  const int fr = lane & 15, fq = lane >> 4;
  const u32x4 zero4 = (u32x4){0u, 0u, 0u, 0u};
#pragma unroll
  for (int p = 0; p < 4; ++p) conv_write_seg(wl, 64 * p + lane + 2, in.own[p], in.nxt[p]);
  conv_write_seg(wl, lane & 1, zero4, (lane & 1) ? in.h0 : (u32x2){0u, 0u});
  *(LAS u32x4*)(wl + CV_U + (lane & 31) * 16) = zero4;
#pragma unroll
  for (int i = 0; i < 4; ++i) *(LAS u32x4*)(wl + CV_U + 512 + i * 1024 + lane * 16) = in.u[i];
  const int o_ = 1 + fr + 8 * fq;
  LAS char* pb = wl + (o_ & 3) * CV_CS + 2 * (o_ & ~3);
  LAS char* qb = wl + CV_U + 16 * (33 + 2 * fr - fq);
  f32x4 acc[8];
#pragma unroll
  for (int g = 0; g < 8; ++g) acc[g] = (f32x4){0.f, 0.f, 0.f, 0.f};
  s16x8 P0[8], Q0[8], P1[8], Q1[8];
  WAIT_L(0); SCHED();
#pragma unroll
  for (int a = 0; a < 8; ++a) { P0[a] = conv_ldp(pb + a * 512); Q0[a] = *(LAS s16x8*)(qb + a * 512); }
  SCHED();
  auto cls = [&](s16x8 (&P)[8], s16x8 (&Q)[8], s16x8 (&Pn)[8], s16x8 (&Qn)[8], LAS char* pn, LAS char* qn) AI {
#pragma unroll
    for (int b = 0; b < 8; ++b) {
      Pn[b] = conv_ldp(pn + b * 512); Qn[b] = *(LAS s16x8*)(qn + b * 512);
#pragma unroll
      for (int a = 0; a + b < 8; ++a) mfma16a(acc[a + b], P[a], Q[b]);
      SCHED();
    }
  };
  for (int r = 0; r < 8; r += 2) {
    if (r == 4) mid();
    cls(P0, Q0, P1, Q1, pb + (r + 1) * 64, qb - (r + 1) * 64);
    const int rn = (r + 2) & 7;
    cls(P1, Q1, P0, Q0, pb + rn * 64, qb - rn * 64);
  }
  MFMA_DRAIN();
#pragma unroll
  for (int e = 0; e < 4; ++e) {
    unsigned zx[2][2];
#pragma unroll
    for (int h = 0; h < 2; ++h) {
      const int g = 2 * e + h;
      const int al = 32 * g + 2 * fr + (fq >> 1);
      const u32x2 kvp = *(LAS u32x2*)(wl + CV_U + 512 + al * 16 + ((fq & 1) ? 0 : 8));
      const float k3 = bflo(kvp[0]), k2 = bfhi(kvp[0]), k1 = bflo(kvp[1]), k0 = bfhi(kvp[1]);
      const float z0 = rbf(acc[g][0]) + rbf(k0 * dv), z1 = rbf(acc[g][1]) + rbf(k1 * dv), z2 = rbf(acc[g][2]) + rbf(k2 * dv), z3 = rbf(acc[g][3]) + rbf(k3 * dv);
      zx[h][0] = pack2(z0, z1); zx[h][1] = pack2(z2, z3);
    }
    const auto r0 = __builtin_amdgcn_permlane16_swap(zx[0][0], zx[1][0], false, false);
    const auto r1 = __builtin_amdgcn_permlane16_swap(zx[0][1], zx[1][1], false, false);
    st16(rws, WS_ZT + ch_off(ch) + (unsigned)(256 * (2 * e + (fq & 1)) + 16 * fr + 8 * (fq >> 1)) * 2u, (u32x4){r0[0], r1[0], r0[1], r1[1]});
  }
}

constexpr int L_STG = 8192, L_ZS = L_STG + 9216, L_RING = 4 * CV_SIZE;
static_assert(L_ZS + 5120 <= CV_SIZE && CV_U + 4608 <= CV_SIZE && L_RING + 65536 <= L_PFS, "lds map");

__global__ __launch_bounds__(512, 2) void gated_mix_fused_kernel(const u16* __restrict__ q, const u16* __restrict__ kk, const u16* __restrict__ vv,
                                                    const u16* __restrict__ shk, const u16* __restrict__ ssmk, const u16* __restrict__ ssmD,
                                                    const u16* __restrict__ wg, const u16* __restrict__ wu, const u16* __restrict__ wd,
                                                    const u16* __restrict__ gw, char* ws, u16* __restrict__ out) {
  extern __shared__ __attribute__((aligned(16))) char shm[];
  LAS char* lds = (LAS char*)shm;
  const int tid = threadIdx.x, lane = tid & 63;
  const int wid = __builtin_amdgcn_readfirstlane(tid >> 6);
  const int fr = lane & 15, fq = lane >> 4;
  const int b = blockIdx.x, xcd = b & 7, j = b >> 3;
  const u64 tag = ((hw_dispatch_id() * 0x9E3779B97F4A7C15ull) ^ 0xD1B54A32D192ED03ull) | 1ull;
  const rsrc_t rws = mk_rsrc(ws, WS_BYTES);
  const int rt = (xcd >> 1) * 4 + (j >> 3), ct = (xcd & 1) * 8 + (j & 7);
  const unsigned myfl = WS_FLAG + (unsigned)(rt * 16 + ct) * FL_STRIDE;
  const int ch = b * 4 + (wid & 3);
  const bool light = j < 4;
  const int jj = light ? 0 : j - 4;
  const int pm = (xcd & 1) * 4 + (jj & 3), pn = (xcd >> 1) * 7 + (jj >> 2);
  const Args a{q, kk, vv, shk, ssmk, ssmD, wg, wu, wd, gw, ws, out};

  if (tid < 16) *(volatile LAS unsigned*)(lds + L_CTRL + tid * 4) = (light && tid < 8) ? 1u : 0u;
  ConvIn in;
  const u16* hrow = ssmk + (size_t)ch * S_;
  unsigned dvraw = 0;
  auto conv_issue_h = [&]() AI {
#pragma unroll
    for (int p = 0; p < 4; ++p) {
      in.own[p] = *(const u32x4*)(hrow + 8 * (64 * p + lane));
      const int nx = 64 * p + lane + 1;
      in.nxt[p] = *(const u32x2*)(hrow + 8 * (nx < 256 ? nx : 255));
    }
    in.h0 = *(const u32x2*)hrow;
    dvraw = ssmD[ch];
  };
  auto conv_issue_u = [&]() AI {
#pragma unroll
    for (int i = 0; i < 4; ++i) in.u[i] = ld16(rws, lane * 16 + i * 1024, WS_KVT + ch_off(ch));
  };
  auto drain_in = [&]() AI {
    asm volatile("s_waitcnt vmcnt(0)" : "+v"(in.u[0]), "+v"(in.u[1]), "+v"(in.u[2]), "+v"(in.u[3]), "+v"(in.own[0]), "+v"(in.own[1]), "+v"(in.own[2]), "+v"(in.own[3]),
                 "+v"(in.nxt[0]), "+v"(in.nxt[1]), "+v"(in.nxt[2]), "+v"(in.nxt[3]), "+v"(in.h0), "+v"(dvraw) :: "memory");
  };
  u64 kv0 = 0;
  if (light) {
    light_unit(lds, a, rws, tag, xcd * 4 + j);
    conv_issue_h();
    flag_wait<4>(kv0, ws, myfl + FL_KV, 32, tag);
    conv_issue_u();
    drain_in();
  } else {
    f32x4 acc1[4][8];
    gemm1_main(acc1, kv0, myfl + FL_KV, lds, a, pm, pn, jj & 3, jj >> 2);
    flag_wait<4>(kv0, ws, myfl + FL_KV, 32, tag);
    conv_issue_h();
    conv_issue_u();
    SCHED();
    gemm1_epi(acc1, rws, pm, pn);
    if (wid < 4) __builtin_amdgcn_s_setprio(0);
    drain_in();
    flag_set(ws, WS_FLAG + (unsigned)((pm * 2 + (wid >> 2)) * 16 + (lane & 15)) * FL_STRIDE + FL_H + ((pn * 2 + (wid & 1)) * 2 + ((wid >> 1) & 1)) * 8, tag);
  }
  lds_wait4(lds + L_CTRL); lds_wait4(lds + L_CTRL + 16);

  if (wid >= 4) {
    const int w4 = wid - 4;
    const unsigned lo16 = lane * 16;
    const unsigned ha = WS_HF + (unsigned)(w4 * 28) * HF_KS + (unsigned)(rt * 8) * 1024u;
    const rsrc_t rd = mk_rsrc(wd, F_ * D_ * 2);
    unsigned vbs[2];
#pragma unroll
    for (int gp = 0; gp < 2; ++gp)
      vbs[gp] = (unsigned)((lane >> 3) & 3) * 2048u + (((lane & 7) ^ ((((lane >> 4) & 1) | (gp << 1)) << 1)) * 16) + (lane >> 5) * (16 * 2048u);
    LAS char* ring = lds + L_RING + w4 * 16384;
    unsigned bb[4];
    {
      const int q4 = (lane & 15) >> 2, p = lane & 3;
      const int f2 = (q4 >> 1) | ((fq & 1) << 1);
#pragma unroll
      for (int n = 0; n < 4; ++n) bb[n] = (unsigned)(size_t)ring + (8 * fq + q4) * 128 + ((((n ^ f2) << 1) + (p >> 1)) * 16) + (p & 1) * 8;
    }
    s16x8 A[2][8], B[2][4];
    auto ldA = [&](int s, int i) AI { return __builtin_bit_cast(s16x8, ld16(rws, lo16 + (i & 3) * 1024, ha + (unsigned)s * HF_KS + (i >> 2) * 4096)); };
    auto dmaB = [&](int s, int slot) AI {
#pragma unroll
      for (int g = 0; g < 4; ++g) blds16(rd, ring + slot * 4096 + g * 1024, vbs[g & 1], (unsigned)((28 * w4 + s) * 32 + 4 * g) * 2048u + ct * 128);
    };
    auto ldB = [&](const int slot, int n) AI {
      return slot == 0 ? tr_read8_asm<0, 512>(bb[n]) : slot == 1 ? tr_read8_asm<4096, 512>(bb[n]) : slot == 2 ? tr_read8_asm<8192, 512>(bb[n]) : tr_read8_asm<12288, 512>(bb[n]);
    };
    const unsigned hfo = myfl + FL_H + w4 * 224;
    u64 rdy = 0; int nxt = 0;
    auto poll_eval = [&](u64 v) AI {
      rdy = __ballot(v == tag);
      __builtin_amdgcn_fence(__ATOMIC_ACQUIRE, "wavefront");
    };
    auto need = [&](const int upto) AI {
      const u64 mask = (1ull << (4 * upto)) - 1ull;
      for (unsigned spins = 0; (rdy & mask) != mask && spins < (1u << 16); ++spins) {
        if (spins) __builtin_amdgcn_s_sleep(4);
        poll_eval(flag_peek(ws, hfo, 28));
      }
    };
    auto next = [&]() AI -> int { return nxt++; };
    dmaB(0, 0); dmaB(1, 1); dmaB(2, 2);
    need(2);
    WAIT_V(0);
    __builtin_amdgcn_s_setprio(3);
    const int o0 = next(), o1 = next();
    int q2 = next(), q3;
#pragma unroll
    for (int i = 0; i < 8; ++i) A[0][i] = ldA(o0, i);
#pragma unroll
    for (int i = 0; i < 8; ++i) A[1][i] = ldA(o1, i);
    f32x4 acc[8][4];
#pragma unroll
    for (int n = 0; n < 4; ++n) B[0][n] = ldB(0, n);
    SCHED();
    auto step = [&](const int sq, const bool first, const bool pre3, const bool pre2, const bool pre1) AI {
      const int sl = sq & 1;
      if (pre3) q3 = next();
      WAIT_L(0);
      if (pre3) dmaB(q3, (sq + 3) & 3);
      if (pre1) {
        if (pre3) { WAIT_V(24); } else if (pre2) { WAIT_V(20); } else { WAIT_V(8); }
#pragma unroll
        for (int n = 0; n < 4; ++n) B[sl ^ 1][n] = ldB((sq + 1) & 3, n);
      }
      SCHED();
#pragma unroll
      for (int i = 0; i < 8; ++i) {
#pragma unroll
        for (int n = 0; n < 4; ++n) { if (first) mfma16z(acc[i][n], B[sl][n], A[sl][i]); else mfma16a(acc[i][n], B[sl][n], A[sl][i]); }
        if (pre2) A[sl][i] = ldA(q2, i);
        SCHED();
      }
      q2 = q3;
    };
    u64 pv = flag_peek(ws, hfo, 28);
    step(0, true, true, true, true); step(1, false, true, true, true);
    poll_eval(pv);
    step(2, false, true, true, true); step(3, false, true, true, true);
    for (int s = 4; s < 24; s += 4) {
      if (s == 20) *(volatile LAS unsigned*)(lds + L_CTRL + 48 + w4 * 4) = 1u;
      const bool look = (rdy & 0xfffffffull) != 0xfffffffull;
      if (look) pv = flag_peek(ws, hfo, 28);
      step(0, false, true, true, true); step(1, false, true, true, true);
      if (look) poll_eval(pv);
      need((s >> 2) + 2);
      step(2, false, true, true, true); step(3, false, true, true, true);
    }
    step(0, false, true, true, true); step(1, false, false, true, true); step(2, false, false, false, true); step(3, false, false, false, false);
    MFMA_DRAIN();
    lds_wait4(lds + L_CTRL + 32);
#pragma unroll
    for (int o = 0; o < 4; ++o) {
      if (o != w4) {
        const int d = (o < w4) ? o : o - 1;
        LAS char* dst = (d < 2) ? ring + d * 8192 : lds + w4 * CV_SIZE;
#pragma unroll
        for (int i2 = 0; i2 < 2; ++i2)
#pragma unroll
          for (int n = 0; n < 4; ++n) *(LAS f32x4*)(dst + (i2 * 4 + n) * 1024 + lo16) = acc[o * 2 + i2][n];
      }
    }
    f32x4 fin[2][4];
#pragma unroll
    for (int o = 0; o < 4; ++o)
      if (o == w4) {
#pragma unroll
        for (int i2 = 0; i2 < 2; ++i2)
#pragma unroll
          for (int n = 0; n < 4; ++n) fin[i2][n] = acc[o * 2 + i2][n];
      }
    WAIT_L(0);
    BAR();
    asm volatile("" ::: "memory");
#pragma unroll
    for (int k = 1; k < 4; ++k) {
      const int sw = (w4 + k) & 3;
      const int d = (w4 < sw) ? w4 : w4 - 1;
      LAS char* src = (d < 2) ? lds + L_RING + sw * 16384 + d * 8192 : lds + sw * CV_SIZE;
#pragma unroll
      for (int i2 = 0; i2 < 2; ++i2)
#pragma unroll
        for (int n = 0; n < 4; ++n) fin[i2][n] += *(LAS f32x4*)(src + (i2 * 4 + n) * 1024 + lo16);
    }
    LAS char* sg = lds + w4 * CV_SIZE + L_STG;
#pragma unroll
    for (int i2 = 0; i2 < 2; ++i2) {
      const int row = rt * 128 + (w4 * 2 + i2) * 16 + fr;
      const float og = *(LAS float*)(sg + 8192 + i2 * 256 + lane * 4);
      unsigned ow[4][2];
#pragma unroll
      for (int n = 0; n < 4; ++n) {
        const f32x4 ga = *(LAS f32x4*)(sg + (i2 * 4 + n) * 1024 + lo16);
        float r4[4];
#pragma unroll
        for (int r = 0; r < 4; ++r) r4[r] = ga[r] + rbf(og * rbf(fin[i2][n][r]));
        ow[n][0] = pack2(r4[0], r4[1]); ow[n][1] = pack2(r4[2], r4[3]);
      }
#pragma unroll
      for (int n2 = 0; n2 < 2; ++n2) {
        const auto r0 = __builtin_amdgcn_permlane16_swap(ow[2 * n2][0], ow[2 * n2 + 1][0], false, false);
        const auto r1 = __builtin_amdgcn_permlane16_swap(ow[2 * n2][1], ow[2 * n2 + 1][1], false, false);
        *(u32x4*)(out + (size_t)row * D_ + ct * 64 + n2 * 32 + (fq & 1) * 16 + (fq >> 1) * 8) = (u32x4){r0[0], r1[0], r0[1], r1[1]};
      }
    }
  } else {
    const float dv = bflo(dvraw);
    u32x4 qr[4], gx[2][2], gwv[2];
#pragma unroll
    for (int i = 0; i < 4; ++i) qr[i] = (u32x4){0u, 0u, 0u, 0u};
    gx[0][0] = gx[0][1] = gx[1][0] = gx[1][1] = gwv[0] = gwv[1] = (u32x4){0u, 0u, 0u, 0u};
    auto side_loads = [&]() AI {
#pragma unroll
    for (int i = 0; i < 4; ++i) qr[i] = *(const u32x4*)(q + (size_t)(rt * 128 + wid * 32 + i * 8 + (lane >> 3)) * D_ + ct * 64 + (lane & 7) * 8);
#pragma unroll
    for (int r = 0; r < 2; ++r) {
      const u16* xr = q + (size_t)(b * 8 + wid * 2 + r) * D_ + lane * 8;
      gx[r][0] = *(const u32x4*)xr; gx[r][1] = *(const u32x4*)(xr + 512);
    }
    gwv[0] = *(const u32x4*)(gw + lane * 8); gwv[1] = *(const u32x4*)(gw + 512 + lane * 8);
    };
    SCHED();
    conv_wave(lds + wid * CV_SIZE, in, dv, rws, ch, side_loads);
    {
      float sg2[2];
#pragma unroll
      for (int r = 0; r < 2; ++r) {
        float s = 0.f;
#pragma unroll
        for (int e = 0; e < 4; ++e) {
          s += rbf(bflo(gx[r][0][e]) * bflo(gwv[0][e])) + rbf(bfhi(gx[r][0][e]) * bfhi(gwv[0][e]));
          s += rbf(bflo(gx[r][1][e]) * bflo(gwv[1][e])) + rbf(bfhi(gx[r][1][e]) * bfhi(gwv[1][e]));
        }
#pragma unroll
        for (int o = 32; o >= 1; o >>= 1) s += __shfl_xor(s, o);
        sg2[r] = s;
      }
      const float s = rbf((lane & 1) ? sg2[1] : sg2[0]);
      const float g = rbf(1.0f / (1.0f + __expf(-s)));
      __builtin_amdgcn_raw_buffer_store_b32(__float_as_uint(g), rws, WS_G + (b * 8 + wid * 2 + (lane & 1)) * 4, 0, SC1);
    }
    WAIT_V(0);
    flag_set(ws, WS_FLAG + ((lane & 16) ? (unsigned)((b >> 4) * 16 + (lane & 15)) * FL_STRIDE + FL_G + ((b & 15) * 4 + wid) * 8
                                        : (unsigned)((lane & 15) * 16 + (ch >> 6)) * FL_STRIDE + FL_Z + (ch & 63) * 8), tag);
    *(volatile LAS unsigned*)(lds + L_CTRL + 32 + wid * 4) = 1u;
    LAS char* sg = lds + wid * CV_SIZE + L_STG;
    LAS char* zs = lds + wid * CV_SIZE + L_ZS;
#pragma unroll
    for (int i = 0; i < 4; ++i) *(LAS u32x4*)(sg + (i * 8 + (lane >> 3)) * 144 + (lane & 7) * 16) = qr[i];
    WAIT_L(0);
    u32x2 qv[2][4];
#pragma unroll
    for (int i2 = 0; i2 < 2; ++i2)
#pragma unroll
      for (int n = 0; n < 4; ++n) qv[i2][n] = *(LAS u32x2*)(sg + (i2 * 16 + fr) * 144 + n * 32 + fq * 8);
    lds_wait4<16>(lds + L_CTRL + 48);
    {
      const unsigned tl = (unsigned)tag, th = (unsigned)(tag >> 32);
      for (unsigned spins = 0; spins < (1u << 16); ++spins) {
        const u32x4 v = ld16(rws, lane * 16, myfl + FL_G);
        if (__all(v[0] == tl && v[1] == th && v[2] == tl && v[3] == th)) break;
        __builtin_amdgcn_s_sleep(16);
      }
      asm volatile("" ::: "memory");
      __builtin_amdgcn_fence(__ATOMIC_ACQUIRE, "wavefront");
    }
    float gv[2];
    {
      u32x4 zr[4];
#pragma unroll
      for (int i2 = 0; i2 < 2; ++i2) gv[i2] = __uint_as_float(__builtin_amdgcn_raw_buffer_load_b32(rws, (rt * 128 + (wid * 2 + i2) * 16 + fr) * 4, WS_G, SC1));
#pragma unroll
      for (int i = 0; i < 4; ++i) zr[i] = ld16(rws, (unsigned)(ct * 65 + i * 16 + (lane >> 2)) * 4096u + (lane & 3) * 16, WS_ZT + (unsigned)(rt * 128 + wid * 32) * 2u);
#pragma unroll
      for (int i = 0; i < 4; ++i) *(LAS u32x4*)(zs + (i * 16 + (lane >> 2)) * 80 + (lane & 3) * 16) = zr[i];
    }
    WAIT_L(0);
    const int q4 = (lane & 15) >> 2, p = lane & 3;
#pragma unroll
    for (int i2 = 0; i2 < 2; ++i2) {
      const float g = gv[i2];
#pragma unroll
      for (int n = 0; n < 4; ++n) {
        const s16x4 zz = __builtin_amdgcn_ds_read_tr16_b64_v4i16((LAS s16x4*)(zs + (16 * n + 4 * fq + q4) * 80 + (i2 * 16 + 4 * p) * 2));
        const u32x2 zu = __builtin_bit_cast(u32x2, zz);
        const unsigned q01 = qv[i2][n][0], q23 = qv[i2][n][1];
        f32x4 ga;
        ga[0] = rbf(g * rbf(bflo(q01) * bflo(zu[0]))); ga[1] = rbf(g * rbf(bfhi(q01) * bfhi(zu[0])));
        ga[2] = rbf(g * rbf(bflo(q23) * bflo(zu[1]))); ga[3] = rbf(g * rbf(bfhi(q23) * bfhi(zu[1])));
        *(LAS f32x4*)(sg + (i2 * 4 + n) * 1024 + lane * 16) = ga;
      }
      *(LAS float*)(sg + 8192 + i2 * 256 + lane * 4) = rbf(1.0f - g);
    }
    WAIT_L(0);
    BAR();
    if (wid < 3) *(u32x4*)(ws + myfl + (wid * 64 + lane) * 16) = (u32x4){0u, 0u, 0u, 0u};
  }
}

extern "C" void kernel_launch(void* const* d_in, const int* in_sizes, int n_in, void* d_out, int out_size, void* d_ws, size_t ws_size,
                              hipStream_t stream) {
  const u16* q = (const u16*)d_in[0]; const u16* kk = (const u16*)d_in[1]; const u16* vv = (const u16*)d_in[2];
  const u16* shk = (const u16*)d_in[3]; const u16* ssmk = (const u16*)d_in[4]; const u16* ssmD = (const u16*)d_in[5];
  const u16* wg = (const u16*)d_in[7]; const u16* wu = (const u16*)d_in[8]; const u16* wd = (const u16*)d_in[9];
  const u16* gw = (const u16*)d_in[10];
  constexpr int LDS1 = 163840;
  (void)hipFuncSetAttribute((const void*)gated_mix_fused_kernel, hipFuncAttributeMaxDynamicSharedMemorySize, LDS1);
  gated_mix_fused_kernel<<<256, 512, LDS1, stream>>>(q, kk, vv, shk, ssmk, ssmD, wg, wu, wd, gw, (char*)d_ws, (u16*)d_out);
}
```

```cpp
#include <hip/hip_runtime.h>
#include <hip/hip_bf16.h>

typedef unsigned short u16;
typedef unsigned long long u64;
typedef short s16x4 __attribute__((ext_vector_type(4)));
typedef short s16x8 __attribute__((ext_vector_type(8)));
typedef __bf16 bfx2 __attribute__((ext_vector_type(2)));
typedef float f32x4 __attribute__((ext_vector_type(4)));
typedef unsigned u32x4 __attribute__((ext_vector_type(4)));
typedef unsigned u32x2 __attribute__((ext_vector_type(2)));
typedef __amdgpu_buffer_rsrc_t rsrc_t;
#define LAS __attribute__((address_space(3)))
#define GAS __attribute__((address_space(1)))
#define DEV __device__ __forceinline__
#define AI __attribute__((always_inline))
extern "C" __device__ u64 hw_dispatch_id() __asm("llvm.amdgcn.dispatch.id");

constexpr int S_ = 2048, D_ = 1024, F_ = 3584;
constexpr unsigned WS_HF = 0;
constexpr unsigned HF_KS = 129 * 1024;
constexpr unsigned WS_KVT = 16u << 20;
constexpr unsigned WS_ZT = 21u << 20;
constexpr unsigned WS_G = 26u << 20;
constexpr unsigned WS_FLAG = 27u << 20;
constexpr unsigned WS_BYTES = 28u << 20;
__device__ constexpr unsigned ch_off(unsigned ch) { return (ch + (ch >> 6)) * 4096u; }
constexpr int FL_H = 0, FL_G = 1024, FL_Z = 1536, FL_KV = 2048;
constexpr unsigned FL_STRIDE = 4096;
constexpr int SC1 = 16;
constexpr int L_PFS = 163840 - 512, L_CTRL = 163840 - 64;

DEV float bflo(unsigned w) { return __uint_as_float(w << 16); }
DEV float bfhi(unsigned w) { return __uint_as_float(w & 0xffff0000u); }
DEV unsigned pack2(float lo, float hi) { bfx2 p; p[0] = (__bf16)lo; p[1] = (__bf16)hi; return __builtin_bit_cast(unsigned, p); }
DEV float rbf(float f) { return (float)(__bf16)f; }
DEV void mfma16a(f32x4& c, s16x8 a, s16x8 b) {
  asm volatile("v_mfma_f32_16x16x32_bf16 %0, %1, %2, %0" : "+v"(c) : "v"(a), "v"(b));
}
DEV void mfma16z(f32x4& c, s16x8 a, s16x8 b) {
  asm volatile("v_mfma_f32_16x16x32_bf16 %0, %1, %2, 0" : "=&v"(c) : "v"(a), "v"(b));
}
template <int OFF, int D2>
DEV s16x8 tr_read8_asm(unsigned addr) {
  s16x4 lo, hi;
  asm volatile("ds_read_b64_tr_b16 %0, %2 offset:%3\n\tds_read_b64_tr_b16 %1, %2 offset:%4" : "=&v"(lo), "=&v"(hi) : "v"(addr), "i"(OFF), "i"(OFF + D2));
  return __builtin_shufflevector(lo, hi, 0, 1, 2, 3, 4, 5, 6, 7);
}
template <int OFF>
DEV s16x8 ds_read16_asm(unsigned addr) {
  s16x8 r;
  asm volatile("ds_read_b128 %0, %1 offset:%2" : "=v"(r) : "v"(addr), "i"(OFF));
  return r;
}
template <int V> struct IC { static constexpr int value = V; };
DEV rsrc_t mk_rsrc(const void* p, unsigned bytes) { return __builtin_amdgcn_make_buffer_rsrc((void*)p, 0, bytes, 0x00020000); }
DEV void blds16(rsrc_t r, LAS char* l, unsigned voff, unsigned soff) {
  __builtin_amdgcn_raw_ptr_buffer_load_lds(r, (LAS void*)l, 16, voff, soff, 0, 0);
}
DEV void blds4(rsrc_t r, LAS char* l, unsigned voff, unsigned soff) {
  __builtin_amdgcn_raw_ptr_buffer_load_lds(r, (LAS void*)l, 4, voff, soff, 0, 0);
}
DEV u32x4 ld16(rsrc_t r, unsigned voff, unsigned soff) { return __builtin_amdgcn_raw_buffer_load_b128(r, voff, soff, SC1); }
DEV void st16(rsrc_t r, unsigned voff, u32x4 v) { __builtin_amdgcn_raw_buffer_store_b128(v, r, voff, 0, SC1); }
#define WAIT_V(n) asm volatile("s_waitcnt vmcnt(" #n ")" ::: "memory")
#define WAIT_L(n) asm volatile("s_waitcnt lgkmcnt(" #n ")" ::: "memory")
#define BAR() __builtin_amdgcn_s_barrier()
#define SCHED() __builtin_amdgcn_sched_barrier(0)
#define MFMA_DRAIN() do { asm volatile("s_nop 7\n\ts_nop 7" ::: "memory"); SCHED(); } while (0)

DEV void flag_set(char* ws, unsigned off, u64 tag) { __hip_atomic_store((GAS u64*)(ws + off), tag, __ATOMIC_RELAXED, __HIP_MEMORY_SCOPE_AGENT); }
DEV u64 flag_peek(char* ws, unsigned off, int n) {
  const int lane = threadIdx.x & 63;
  return __hip_atomic_load((GAS u64*)(ws + off + 8 * (lane < n ? lane : 0)), __ATOMIC_RELAXED, __HIP_MEMORY_SCOPE_AGENT);
}
template <int SLP>
DEV void flag_wait(u64 first, char* ws, unsigned off, int n, u64 tag) {
  u64 v = first;
  for (unsigned spins = 0; spins < (1u << 16); ++spins) {
    if (__all(v == tag)) break;
    __builtin_amdgcn_s_sleep(SLP);
    v = flag_peek(ws, off, n);
  }
  __builtin_amdgcn_fence(__ATOMIC_ACQUIRE, "wavefront");
}
template <int SLP = 1>
DEV void lds_wait4(LAS char* p) {
  for (unsigned spins = 0; spins < (1u << 22); ++spins) {
    const u32x4 dn = *(volatile LAS u32x4*)p;
    if (__builtin_amdgcn_readfirstlane(dn[0] & dn[1] & dn[2] & dn[3]) != 0u) break;
    __builtin_amdgcn_s_sleep(SLP);
  }
  asm volatile("" ::: "memory");
}
DEV int tile_of_block(int b) { const int xcd = b & 7, j = b >> 3; return ((xcd >> 1) * 4 + (j >> 3)) * 16 + (xcd & 1) * 8 + (j & 7); }

struct Args {
  const u16 *q, *kk, *vv, *shk, *ssmk, *ssmD, *wg, *wu, *wd, *gw;
  char* ws; u16* out;
};

DEV void gemm1_main(f32x4 (&acc)[4][8], u64& kvpeek, unsigned kvoff, LAS char* lds, const Args& a, int pm, int pn, int im, int in_) {
  char* ws = a.ws;
  const int tid = threadIdx.x, lane = tid & 63;
  const int wid = __builtin_amdgcn_readfirstlane(tid >> 6);
  const int wr = wid >> 1, wc = wid & 1;
  const int fr = lane & 15, fq = lane >> 4;
  unsigned va[2], vb[2];
#pragma unroll
  for (int e = 0; e < 2; ++e) {
    const int rl = lane >> 3, p = lane & 7;
    const int sw = (e << 2) | (lane >> 4);
    va[e] = (unsigned)(rl * 2048 + ((p ^ sw) * 16));
    const int kl = lane >> 4, pb = lane & 15;
    const int swb = (kl | (e << 2)) << 1;
    vb[e] = (unsigned)(kl * (F_ * 2) + (pb ^ swb) * 16);
  }
  const rsrc_t rx = mk_rsrc(a.q, S_ * D_ * 2);
  const rsrc_t rw = mk_rsrc((wid < 4) ? a.wg : a.wu, D_ * F_ * 2);
  const unsigned xa_base = (unsigned)(pm * 256 + wid * 32) * 2048u;
  const unsigned wb_base = (unsigned)((wid & 3) * 16) * (F_ * 2) + pn * 256;
  LAS char* ldsA = lds + wid * 4096;
  LAS char* ldsB = lds + 65536 + wid * 4096;
  auto dma = [&](int kt, int st, int d) AI {
    const int i = d >> 1;
    if (d & 1) blds16(rw, ldsB + st * 32768 + i * 1024, vb[i >> 1], wb_base + (kt * 64 + i * 4) * (F_ * 2));
    else blds16(rx, ldsA + st * 32768 + i * 1024, va[i & 1], xa_base + i * (8 * 2048) + kt * 128);
  };
#pragma unroll
  for (int d = 0; d < 8; ++d) dma(0, 0, d);
  SCHED(); BAR(); SCHED();
#pragma unroll
  for (int d = 0; d < 8; ++d) dma(1, 1, d);
  SCHED();
  rsrc_t rp; unsigned vp, pstep;
  if (wid < 4) {
    rp = mk_rsrc((im >> 1) ? a.wu : a.wg, D_ * F_ * 2);
    vp = (unsigned)(wid * 16 + (lane & 15)) * (F_ * 2) + pn * 256 + (im & 1) * 128; pstep = 64 * F_ * 2;
  } else {
    rp = rx;
    int r = in_ * 37 + (wid - 4) * 10 + ((lane & 15) < 10 ? (lane & 15) : 9); r = r > 255 ? 255 : r;
    vp = (unsigned)(pm * 256 + r) * 2048u; pstep = 128;
  }
  vp += (unsigned)(lane >> 4) * 32u;
  auto pf = [&](int kt) AI { blds4(rp, lds + L_PFS, vp, kt * pstep); };
  const unsigned lds0 = (unsigned)(size_t)lds;
  unsigned bx[2];
#pragma unroll
  for (int ks = 0; ks < 2; ++ks) bx[ks] = lds0 + (wr * 64 + fr) * 128 + (((ks * 4 + fq) ^ (fr >> 1)) * 16);
  unsigned bw[4];
  {
    const int q = (lane & 15) >> 2, p = lane & 3;
    const int sw = (q | ((fq & 1) << 2)) << 1;
#pragma unroll
    for (int j = 0; j < 4; ++j) bw[j] = lds0 + 65536 + (8 * fq + q) * 256 + (((wc * 8 + j * 2 + (p >> 1)) ^ sw) * 16) + (p & 1) * 8;
  }
  auto ldx = [&](auto ST, auto KS, const int i) AI -> s16x8 {
    constexpr int o = decltype(ST)::value * 32768; const unsigned p = bx[decltype(KS)::value];
    return i == 0 ? ds_read16_asm<o>(p) : i == 1 ? ds_read16_asm<o + 2048>(p) : i == 2 ? ds_read16_asm<o + 4096>(p) : ds_read16_asm<o + 6144>(p);
  };
  auto ldw = [&](auto ST, auto KS, const int n) AI -> s16x8 {
    constexpr int o = decltype(ST)::value * 32768 + decltype(KS)::value * 8192;
    return n < 4 ? tr_read8_asm<o, 1024>(bw[n & 3]) : tr_read8_asm<o + 16384, 1024>(bw[n & 3]);
  };
  s16x8 xa[4], xb[4], w[8];
  pf(2);
  WAIT_V(9); BAR(); SCHED();
#pragma unroll
  for (int i = 0; i < 4; ++i) xa[i] = ldx(IC<0>{}, IC<0>{}, i);
#pragma unroll
  for (int n = 0; n < 8; ++n) w[n] = ldw(IC<0>{}, IC<0>{}, n);
  pf(3);
  WAIT_L(0);
  SCHED();

  auto tile_body = [&](auto ST, const int kt, const bool pre1, const bool pre2, const int hook) AI {
    constexpr int st = decltype(ST)::value;
#pragma unroll
    for (int n = 0; n < 8; ++n) {
      if (pre2) { if (n == 2) __builtin_amdgcn_s_setprio(1); if (n == 6) __builtin_amdgcn_s_setprio(0); }
      if (n == 6) WAIT_L(15);
#pragma unroll
      for (int i = 0; i < 4; ++i) {
        if (n == 0) { if (i == 0) WAIT_L(13); if (i == 1) WAIT_L(10); if (i == 2) WAIT_L(7); if (i == 3) WAIT_L(4); }
        if (hook == 1) mfma16z(acc[i][n], w[n], xa[i]); else mfma16a(acc[i][n], w[n], xa[i]);
      }
      w[n] = ldw(ST, IC<1>{}, n);
      if (n < 4) xb[n] = ldx(ST, IC<1>{}, n);
      if (n == 3 && hook <= 1) pf(kt + 4);
      if (n == 3 && hook == 5) kvpeek = flag_peek(ws, kvoff, 32);
      SCHED();
    }
#pragma unroll
    for (int n = 0; n < 2; ++n) {
#pragma unroll
      for (int i = 0; i < 4; ++i) {
        if (n == 0) { if (i == 0) WAIT_L(15); if (i == 1) WAIT_L(14); if (i == 2) WAIT_L(11); if (i == 3) WAIT_L(8); }
        mfma16a(acc[i][n], w[n], xb[i]);
      }
    }
    SCHED();
    WAIT_L(0);
    if (pre1) {
      if (hook == 1) { WAIT_V(3); } else if (hook == 7) { WAIT_V(0); } else { WAIT_V(1); }
      BAR(); SCHED();
      if (pre2) __builtin_amdgcn_s_setprio(3);
      else if (wid < 4) __builtin_amdgcn_s_setprio(2);
      else __builtin_amdgcn_s_setprio(1);
      w[0] = ldw(IC<(st ^ 1)>{}, IC<0>{}, 0); w[1] = ldw(IC<(st ^ 1)>{}, IC<0>{}, 1);
      if (pre2) { dma(kt + 2, st, 0); dma(kt + 2, st, 1); }
      SCHED();
    }
#pragma unroll
    for (int n = 2; n < 8; ++n) {
      if (pre2 && n == 6) __builtin_amdgcn_s_setprio(2);
#pragma unroll
      for (int i = 0; i < 4; ++i) mfma16a(acc[i][n], w[n], xb[i]);
      if (pre1) {
        w[n] = ldw(IC<(st ^ 1)>{}, IC<0>{}, n);
        if (n - 2 < 4) xa[n - 2] = ldx(IC<(st ^ 1)>{}, IC<0>{}, n - 2);
        if (pre2) dma(kt + 2, st, n);
      }
      SCHED();
    }
  };
  tile_body(IC<0>{}, 0, true, true, 1);
  tile_body(IC<1>{}, 1, true, true, 0);
  for (int kt = 2; kt < 12; kt += 2) {
    tile_body(IC<0>{}, kt, true, true, 0);
    tile_body(IC<1>{}, kt + 1, true, true, 0);
  }
  tile_body(IC<0>{}, 12, true, true, 7);
  tile_body(IC<1>{}, 13, true, true, 7);
  tile_body(IC<0>{}, 14, true, false, 5);
  tile_body(IC<1>{}, 15, false, false, 7);
  *(volatile LAS unsigned*)(lds + L_CTRL + wid * 4) = 1u;
  MFMA_DRAIN();
}

DEV void light_unit(LAS char* lds, const Args& a, rsrc_t rws, u64 tag, int cu) {
  char* ws = a.ws;
  const int tid = threadIdx.x, lane = tid & 63;
  const int wid = __builtin_amdgcn_readfirstlane(tid >> 6);
  const int cb = cu & 7, tblk = (cu >> 3) * 8 + wid;
  const int kch = cb * 128 + lane * 2;
  LAS char* wl = lds + wid * (128 * 144);
  {
    const int hj = cu & 3;
    if (wid == 7) {
      if (hj == 0) { blds4(mk_rsrc(a.wg, D_ * F_ * 2), lds + L_PFS, lane * 65536, 0); blds4(mk_rsrc(a.wg, D_ * F_ * 2), lds + L_PFS, lane * 65536, 64 * 65536); blds4(mk_rsrc(a.q, S_ * D_ * 2), lds + L_PFS, lane * 65536, 0); }
      if (hj == 1) { blds4(mk_rsrc(a.wu, D_ * F_ * 2), lds + L_PFS, lane * 65536, 0); blds4(mk_rsrc(a.wu, D_ * F_ * 2), lds + L_PFS, lane * 65536, 64 * 65536); blds4(mk_rsrc(a.out, S_ * D_ * 2), lds + L_PFS, lane * 65536, 0); }
      if (hj == 2) { blds4(mk_rsrc(a.wd, D_ * F_ * 2), lds + L_PFS, lane * 65536, 0); blds4(mk_rsrc(a.wd, D_ * F_ * 2), lds + L_PFS, lane * 65536, 64 * 65536); blds4(mk_rsrc(a.ssmk, S_ * D_ * 2), lds + L_PFS, lane * 65536, 0); }
      if (hj == 3) {
        blds4(rws, lds + L_PFS, lane * HF_KS + 131072, WS_HF); blds4(rws, lds + L_PFS, (lane < 48 ? lane : 47) * HF_KS + 131072, WS_HF + 64 * HF_KS);
        blds4(rws, lds + L_PFS, (lane & 15) * (65 * 4096u) + 64 * 4096u + ((lane & 16) ? WS_ZT : WS_KVT), 0);
        blds4(rws, lds + L_PFS, (lane & 15) * 65536 + 4032, WS_FLAG);
      }
    }
  }
  const u32x4 shw = *(const u32x4*)(a.shk + kch * 4);
#pragma unroll
  for (int hf = 0; hf < 2; ++hf) {
    const int t0 = tblk * 64 + hf * 32;
    unsigned kr[35], vr[32];
#pragma unroll
    for (int jx = 0; jx < 35; ++jx) { const int t = t0 - 3 + jx; kr[jx] = *(const unsigned*)(a.kk + (size_t)(t < 0 ? 0 : t) * D_ + kch); }
#pragma unroll
    for (int jx = 0; jx < 32; ++jx) vr[jx] = *(const unsigned*)(a.vv + (size_t)(t0 + jx) * D_ + kch);
    SCHED();
#pragma unroll
    for (int c = 0; c < 2; ++c) {
      const unsigned s01 = shw[c * 2], s23 = shw[c * 2 + 1];
      const float w0 = bflo(s01), w1 = bfhi(s01), w2 = bflo(s23), w3 = bfhi(s23);
#pragma unroll
      for (int sb = 0; sb < 4; ++sb) {
        float kf[11];
#pragma unroll
        for (int jx = 0; jx < 11; ++jx) { kf[jx] = c ? bfhi(kr[sb * 8 + jx]) : bflo(kr[sb * 8 + jx]); if (sb == 0 && jx < 3 && t0 == 0) kf[jx] = 0.f; }
        float o[8];
#pragma unroll
        for (int jx = 0; jx < 8; ++jx) o[jx] = (kf[jx] * w3 + kf[jx + 1] * w2 + kf[jx + 2] * w1 + kf[jx + 3] * w0) * (c ? bfhi(vr[sb * 8 + jx]) : bflo(vr[sb * 8 + jx]));
        *(LAS u32x4*)(wl + (lane * 2 + c) * 144 + (hf * 4 + sb) * 16) = (u32x4){pack2(o[7], o[6]), pack2(o[5], o[4]), pack2(o[3], o[2]), pack2(o[1], o[0])};
      }
    }
  }
  WAIT_L(0);
#pragma unroll
  for (int e = 0; e < 16; ++e) {
    const int c = e * 8 + (lane >> 3);
    const u32x4 v = *(LAS u32x4*)(wl + c * 144 + (lane & 7) * 16);
    st16(rws, WS_KVT + ch_off(cb * 128 + c) + tblk * 128 + (lane & 7) * 16, v);
  }
  WAIT_V(0);
  flag_set(ws, WS_FLAG + (unsigned)tile_of_block(cb * 32 + (lane & 31)) * FL_STRIDE + FL_KV + tblk * 8, tag);
  const rsrc_t rd = mk_rsrc(a.wd, F_ * D_ * 2), rk = mk_rsrc(a.ssmk, D_ * S_ * 2);
#pragma unroll
  for (int i = 0; i < 2; ++i) blds4(rk, lds + L_PFS, tid * 128, cu * 131072 + i * 65536);
#pragma unroll
  for (int i = 0; i < 4; ++i) blds4(rd, lds + L_PFS, tid * 128, (i * 32 + cu) * 57344);
  WAIT_V(0);
  __syncthreads();
}

DEV u32x2 geglu4(f32x4 gt, f32x4 up) {
  const float C1 = -2.302208198f, C2 = -2.302208198f * 0.044715f;
  const f32x4 wv = gt * (gt * gt * C2 + C1);
  f32x4 ev;
#pragma unroll
  for (int r = 0; r < 4; ++r) ev[r] = __builtin_amdgcn_exp2f(wv[r]);
  const f32x4 dv = ev + 1.0f;
  f32x4 rv;
#pragma unroll
  for (int r = 0; r < 4; ++r) rv[r] = __builtin_amdgcn_rcpf(dv[r]);
  const f32x4 hv = gt * up * rv;
  return (u32x2){pack2(hv[0], hv[1]), pack2(hv[2], hv[3])};
}
DEV void gemm1_epi(const f32x4 (&acc)[4][8], rsrc_t rws, int pm, int pn) {
  const int lane = threadIdx.x & 63;
  const int wid = __builtin_amdgcn_readfirstlane(threadIdx.x >> 6);
  const int wr = wid >> 1, wc = wid & 1;
  const unsigned off = WS_HF + (unsigned)(pn * 4 + wc * 2) * HF_KS + (unsigned)(pm * 16 + wr * 4) * 1024u + lane * 16;
#pragma unroll
  for (int i = 0; i < 4; ++i)
#pragma unroll
    for (int k2 = 0; k2 < 2; ++k2) {
      const u32x2 h0 = geglu4(acc[i][k2 * 2], acc[i][4 + k2 * 2]), h1 = geglu4(acc[i][k2 * 2 + 1], acc[i][4 + k2 * 2 + 1]);
      st16(rws, off + k2 * HF_KS + i * 1024, (u32x4){h0[0], h0[1], h1[0], h1[1]});
    }
}

constexpr int CV_CS = 4160, CV_U = 4 * CV_CS  , CV_SIZE = 23552;
struct ConvIn { u32x4 own[4]; u32x2 nxt[4], h0; u32x4 u[4]; };

DEV void conv_write_seg(LAS char* wl, int seg, u32x4 a, u32x2 b) {
  const unsigned a01 = __builtin_amdgcn_alignbit(a[1], a[0], 16), a12 = __builtin_amdgcn_alignbit(a[2], a[1], 16),
                 a23 = __builtin_amdgcn_alignbit(a[3], a[2], 16), a34 = __builtin_amdgcn_alignbit(b[0], a[3], 16),
                 a45 = __builtin_amdgcn_alignbit(b[1], b[0], 16);
  LAS char* dst = wl + seg * 16;
  *(LAS u32x4*)(dst) = a;
  *(LAS u32x4*)(dst + CV_CS) = (u32x4){a01, a12, a23, a34};
  *(LAS u32x4*)(dst + 2 * CV_CS) = (u32x4){a[1], a[2], a[3], b[0]};
  *(LAS u32x4*)(dst + 3 * CV_CS) = (u32x4){a12, a23, a34, a45};
}
DEV s16x8 conv_ldp(LAS char* p) {
  volatile LAS s16x4* pp = (volatile LAS s16x4*)p;
  const s16x4 lo = pp[0], hi = pp[1];
  return __builtin_shufflevector(lo, hi, 0, 1, 2, 3, 4, 5, 6, 7);
}

template <class Mid>
DEV void conv_wave(LAS char* wl, const ConvIn& in, float dv, rsrc_t rws, int ch, Mid&& mid) {
  const int lane = threadIdx.x & 63;
  const int fr = lane & 15, fq = lane >> 4;
  const u32x4 zero4 = (u32x4){0u, 0u, 0u, 0u};
#pragma unroll
  for (int p = 0; p < 4; ++p) conv_write_seg(wl, 64 * p + lane + 2, in.own[p], in.nxt[p]);
  conv_write_seg(wl, lane & 1, zero4, (lane & 1) ? in.h0 : (u32x2){0u, 0u});
  *(LAS u32x4*)(wl + CV_U + (lane & 31) * 16) = zero4;
#pragma unroll
  for (int i = 0; i < 4; ++i) *(LAS u32x4*)(wl + CV_U + 512 + i * 1024 + lane * 16) = in.u[i];
  const int o_ = 1 + fr + 8 * fq;
  LAS char* pb = wl + (o_ & 3) * CV_CS + 2 * (o_ & ~3);
  LAS char* qb = wl + CV_U + 16 * (33 + 2 * fr - fq);
  f32x4 acc[8];
#pragma unroll
  for (int g = 0; g < 8; ++g) acc[g] = (f32x4){0.f, 0.f, 0.f, 0.f};
  s16x8 P0[8], Q0[8], P1[8], Q1[8];
  WAIT_L(0); SCHED();
#pragma unroll
  for (int a = 0; a < 8; ++a) { P0[a] = conv_ldp(pb + a * 512); Q0[a] = *(LAS s16x8*)(qb + a * 512); }
  SCHED();
  auto cls = [&](s16x8 (&P)[8], s16x8 (&Q)[8], s16x8 (&Pn)[8], s16x8 (&Qn)[8], LAS char* pn, LAS char* qn) AI {
#pragma unroll
    for (int b = 0; b < 8; ++b) {
      Pn[b] = conv_ldp(pn + b * 512); Qn[b] = *(LAS s16x8*)(qn + b * 512);
#pragma unroll
      for (int a = 0; a + b < 8; ++a) mfma16a(acc[a + b], P[a], Q[b]);
      SCHED();
    }
  };
  for (int r = 0; r < 8; r += 2) {
    if (r == 4) mid();
    cls(P0, Q0, P1, Q1, pb + (r + 1) * 64, qb - (r + 1) * 64);
    const int rn = (r + 2) & 7;
    cls(P1, Q1, P0, Q0, pb + rn * 64, qb - rn * 64);
  }
  MFMA_DRAIN();
#pragma unroll
  for (int e = 0; e < 4; ++e) {
    unsigned zx[2][2];
#pragma unroll
    for (int h = 0; h < 2; ++h) {
      const int g = 2 * e + h;
      const int al = 32 * g + 2 * fr + (fq >> 1);
      const u32x2 kvp = *(LAS u32x2*)(wl + CV_U + 512 + al * 16 + ((fq & 1) ? 0 : 8));
      const float k3 = bflo(kvp[0]), k2 = bfhi(kvp[0]), k1 = bflo(kvp[1]), k0 = bfhi(kvp[1]);
      const float z0 = rbf(acc[g][0]) + rbf(k0 * dv), z1 = rbf(acc[g][1]) + rbf(k1 * dv), z2 = rbf(acc[g][2]) + rbf(k2 * dv), z3 = rbf(acc[g][3]) + rbf(k3 * dv);
      zx[h][0] = pack2(z0, z1); zx[h][1] = pack2(z2, z3);
    }
    const auto r0 = __builtin_amdgcn_permlane16_swap(zx[0][0], zx[1][0], false, false);
    const auto r1 = __builtin_amdgcn_permlane16_swap(zx[0][1], zx[1][1], false, false);
    st16(rws, WS_ZT + ch_off(ch) + (unsigned)(256 * (2 * e + (fq & 1)) + 16 * fr + 8 * (fq >> 1)) * 2u, (u32x4){r0[0], r1[0], r0[1], r1[1]});
  }
}

constexpr int L_STG = 8192, L_ZS = L_STG + 9216, L_RING = 4 * CV_SIZE;
static_assert(L_ZS + 5120 <= CV_SIZE && CV_U + 4608 <= CV_SIZE && L_RING + 65536 <= L_PFS, "lds map");

__global__ __launch_bounds__(512, 2) void gated_mix_fused_kernel(const u16* __restrict__ q, const u16* __restrict__ kk, const u16* __restrict__ vv,
                                                    const u16* __restrict__ shk, const u16* __restrict__ ssmk, const u16* __restrict__ ssmD,
                                                    const u16* __restrict__ wg, const u16* __restrict__ wu, const u16* __restrict__ wd,
                                                    const u16* __restrict__ gw, char* ws, u16* __restrict__ out) {
  extern __shared__ __attribute__((aligned(16))) char shm[];
  LAS char* lds = (LAS char*)shm;
  const int tid = threadIdx.x, lane = tid & 63;
  const int wid = __builtin_amdgcn_readfirstlane(tid >> 6);
  const int fr = lane & 15, fq = lane >> 4;
  const int b = blockIdx.x, xcd = b & 7, j = b >> 3;
  const u64 tag = ((hw_dispatch_id() * 0x9E3779B97F4A7C15ull) ^ 0xD1B54A32D192ED03ull) | 1ull;
  const rsrc_t rws = mk_rsrc(ws, WS_BYTES);
  const int rt = (xcd >> 1) * 4 + (j >> 3), ct = (xcd & 1) * 8 + (j & 7);
  const unsigned myfl = WS_FLAG + (unsigned)(rt * 16 + ct) * FL_STRIDE;
  const int ch = b * 4 + (wid & 3);
  const bool light = j < 4;
  const int jj = light ? 0 : j - 4;
  const int pm = (xcd & 1) * 4 + (jj & 3), pn = (xcd >> 1) * 7 + (jj >> 2);
  const Args a{q, kk, vv, shk, ssmk, ssmD, wg, wu, wd, gw, ws, out};

  if (tid < 16) *(volatile LAS unsigned*)(lds + L_CTRL + tid * 4) = (light && tid < 8) ? 1u : 0u;
  ConvIn in;
  const u16* hrow = ssmk + (size_t)ch * S_;
  unsigned dvraw = 0;
  const int cl = wid < 4 ? lane : 0, cp = wid < 4 ? 64 : 0;
  auto conv_issue_h = [&]() AI {
#pragma unroll
    for (int p = 0; p < 4; ++p) {
      in.own[p] = *(const u32x4*)(hrow + 8 * (cp * p + cl));
      const int nx = cp * p + cl + 1;
      in.nxt[p] = *(const u32x2*)(hrow + 8 * (nx < 256 ? nx : 255));
    }
    in.h0 = *(const u32x2*)hrow;
    dvraw = ssmD[ch];
  };
  auto conv_issue_u = [&]() AI {
#pragma unroll
    for (int i = 0; i < 4; ++i) in.u[i] = ld16(rws, cl * 16 + i * 16 * cp, WS_KVT + ch_off(ch));
  };
  auto drain_in = [&]() AI {
    asm volatile("s_waitcnt vmcnt(0)" : "+v"(in.u[0]), "+v"(in.u[1]), "+v"(in.u[2]), "+v"(in.u[3]), "+v"(in.own[0]), "+v"(in.own[1]), "+v"(in.own[2]), "+v"(in.own[3]),
                 "+v"(in.nxt[0]), "+v"(in.nxt[1]), "+v"(in.nxt[2]), "+v"(in.nxt[3]), "+v"(in.h0), "+v"(dvraw) :: "memory");
  };
  u64 kv0 = 0;
  if (light) {
    light_unit(lds, a, rws, tag, xcd * 4 + j);
    conv_issue_h();
    flag_wait<4>(kv0, ws, myfl + FL_KV, 32, tag);
    conv_issue_u();
    drain_in();
  } else {
    f32x4 acc1[4][8];
    gemm1_main(acc1, kv0, myfl + FL_KV, lds, a, pm, pn, jj & 3, jj >> 2);
    flag_wait<4>(kv0, ws, myfl + FL_KV, 32, tag);
    conv_issue_h();
    conv_issue_u();
    SCHED();
    gemm1_epi(acc1, rws, pm, pn);
    if (wid < 4) __builtin_amdgcn_s_setprio(0);
    drain_in();
    flag_set(ws, WS_FLAG + (unsigned)((pm * 2 + (wid >> 2)) * 16 + (lane & 15)) * FL_STRIDE + FL_H + ((pn * 2 + (wid & 1)) * 2 + ((wid >> 1) & 1)) * 8, tag);
  }
  lds_wait4(lds + L_CTRL); lds_wait4(lds + L_CTRL + 16);

  if (wid >= 4) {
    const int w4 = wid - 4;
    const unsigned lo16 = lane * 16;
    const unsigned ha = WS_HF + (unsigned)(w4 * 28) * HF_KS + (unsigned)(rt * 8) * 1024u;
    const rsrc_t rd = mk_rsrc(wd, F_ * D_ * 2);
    unsigned vbs[2];
#pragma unroll
    for (int gp = 0; gp < 2; ++gp)
      vbs[gp] = (unsigned)((lane >> 3) & 3) * 2048u + (((lane & 7) ^ ((((lane >> 4) & 1) | (gp << 1)) << 1)) * 16) + (lane >> 5) * (16 * 2048u);
    LAS char* ring = lds + L_RING + w4 * 16384;
    unsigned bb[4];
    {
      const int q4 = (lane & 15) >> 2, p = lane & 3;
      const int f2 = (q4 >> 1) | ((fq & 1) << 1);
#pragma unroll
      for (int n = 0; n < 4; ++n) bb[n] = (unsigned)(size_t)ring + (8 * fq + q4) * 128 + ((((n ^ f2) << 1) + (p >> 1)) * 16) + (p & 1) * 8;
    }
    s16x8 A[2][8], B[2][4];
    auto ldA = [&](int s, int i) AI { return __builtin_bit_cast(s16x8, ld16(rws, lo16 + (i & 3) * 1024, ha + (unsigned)s * HF_KS + (i >> 2) * 4096)); };
    auto dmaB = [&](int s, int slot) AI {
#pragma unroll
      for (int g = 0; g < 4; ++g) blds16(rd, ring + slot * 4096 + g * 1024, vbs[g & 1], (unsigned)((28 * w4 + s) * 32 + 4 * g) * 2048u + ct * 128);
    };
    auto ldB = [&](const int slot, int n) AI {
      return slot == 0 ? tr_read8_asm<0, 512>(bb[n]) : slot == 1 ? tr_read8_asm<4096, 512>(bb[n]) : slot == 2 ? tr_read8_asm<8192, 512>(bb[n]) : tr_read8_asm<12288, 512>(bb[n]);
    };
    const unsigned hfo = myfl + FL_H + w4 * 224;
    u64 rdy = 0; int nxt = 0;
    auto poll_eval = [&](u64 v) AI {
      rdy = __ballot(v == tag);
      __builtin_amdgcn_fence(__ATOMIC_ACQUIRE, "wavefront");
    };
    auto need = [&](const int upto) AI {
      const u64 mask = (1ull << (4 * upto)) - 1ull;
      for (unsigned spins = 0; (rdy & mask) != mask && spins < (1u << 16); ++spins) {
        if (spins) __builtin_amdgcn_s_sleep(4);
        poll_eval(flag_peek(ws, hfo, 28));
      }
    };
    auto next = [&]() AI -> int { return nxt++; };
    dmaB(0, 0); dmaB(1, 1); dmaB(2, 2);
    need(2);
    WAIT_V(0);
    __builtin_amdgcn_s_setprio(3);
    const int o0 = next(), o1 = next();
    int q2 = next(), q3;
#pragma unroll
    for (int i = 0; i < 8; ++i) A[0][i] = ldA(o0, i);
#pragma unroll
    for (int i = 0; i < 8; ++i) A[1][i] = ldA(o1, i);
    f32x4 acc[8][4];
#pragma unroll
    for (int n = 0; n < 4; ++n) B[0][n] = ldB(0, n);
    SCHED();
    auto step = [&](const int sq, const bool first, const bool pre3, const bool pre2, const bool pre1) AI {
      const int sl = sq & 1;
      if (pre3) q3 = next();
      WAIT_L(0);
      if (pre3) dmaB(q3, (sq + 3) & 3);
      if (pre1) {
        if (pre3) { WAIT_V(24); } else if (pre2) { WAIT_V(20); } else { WAIT_V(8); }
#pragma unroll
        for (int n = 0; n < 4; ++n) B[sl ^ 1][n] = ldB((sq + 1) & 3, n);
      }
      SCHED();
#pragma unroll
      for (int i = 0; i < 8; ++i) {
#pragma unroll
        for (int n = 0; n < 4; ++n) { if (first) mfma16z(acc[i][n], B[sl][n], A[sl][i]); else mfma16a(acc[i][n], B[sl][n], A[sl][i]); }
        if (pre2) A[sl][i] = ldA(q2, i);
        SCHED();
      }
      q2 = q3;
    };
    u64 pv = flag_peek(ws, hfo, 28);
    step(0, true, true, true, true); step(1, false, true, true, true);
    poll_eval(pv);
    step(2, false, true, true, true); step(3, false, true, true, true);
    for (int s = 4; s < 24; s += 4) {
      if (s == 20) *(volatile LAS unsigned*)(lds + L_CTRL + 48 + w4 * 4) = 1u;
      const bool look = (rdy & 0xfffffffull) != 0xfffffffull;
      if (look) pv = flag_peek(ws, hfo, 28);
      step(0, false, true, true, true); step(1, false, true, true, true);
      if (look) poll_eval(pv);
      need((s >> 2) + 2);
      step(2, false, true, true, true); step(3, false, true, true, true);
    }
    step(0, false, true, true, true); step(1, false, false, true, true); step(2, false, false, false, true); step(3, false, false, false, false);
    MFMA_DRAIN();
    lds_wait4(lds + L_CTRL + 32);
#pragma unroll
    for (int o = 0; o < 4; ++o) {
      if (o != w4) {
        const int d = (o < w4) ? o : o - 1;
        LAS char* dst = (d < 2) ? ring + d * 8192 : lds + w4 * CV_SIZE;
#pragma unroll
        for (int i2 = 0; i2 < 2; ++i2)
#pragma unroll
          for (int n = 0; n < 4; ++n) *(LAS f32x4*)(dst + (i2 * 4 + n) * 1024 + lo16) = acc[o * 2 + i2][n];
      }
    }
    f32x4 fin[2][4];
#pragma unroll
    for (int o = 0; o < 4; ++o)
      if (o == w4) {
#pragma unroll
        for (int i2 = 0; i2 < 2; ++i2)
#pragma unroll
          for (int n = 0; n < 4; ++n) fin[i2][n] = acc[o * 2 + i2][n];
      }
    WAIT_L(0);
    BAR();
    asm volatile("" ::: "memory");
#pragma unroll
    for (int k = 1; k < 4; ++k) {
      const int sw = (w4 + k) & 3;
      const int d = (w4 < sw) ? w4 : w4 - 1;
      LAS char* src = (d < 2) ? lds + L_RING + sw * 16384 + d * 8192 : lds + sw * CV_SIZE;
#pragma unroll
      for (int i2 = 0; i2 < 2; ++i2)
#pragma unroll
        for (int n = 0; n < 4; ++n) fin[i2][n] += *(LAS f32x4*)(src + (i2 * 4 + n) * 1024 + lo16);
    }
    LAS char* sg = lds + w4 * CV_SIZE + L_STG;
#pragma unroll
    for (int i2 = 0; i2 < 2; ++i2) {
      const int row = rt * 128 + (w4 * 2 + i2) * 16 + fr;
      const float og = *(LAS float*)(sg + 8192 + i2 * 256 + lane * 4);
      unsigned ow[4][2];
#pragma unroll
      for (int n = 0; n < 4; ++n) {
        const f32x4 ga = *(LAS f32x4*)(sg + (i2 * 4 + n) * 1024 + lo16);
        float r4[4];
#pragma unroll
        for (int r = 0; r < 4; ++r) r4[r] = ga[r] + rbf(og * rbf(fin[i2][n][r]));
        ow[n][0] = pack2(r4[0], r4[1]); ow[n][1] = pack2(r4[2], r4[3]);
      }
#pragma unroll
      for (int n2 = 0; n2 < 2; ++n2) {
        const auto r0 = __builtin_amdgcn_permlane16_swap(ow[2 * n2][0], ow[2 * n2 + 1][0], false, false);
        const auto r1 = __builtin_amdgcn_permlane16_swap(ow[2 * n2][1], ow[2 * n2 + 1][1], false, false);
        *(u32x4*)(out + (size_t)row * D_ + ct * 64 + n2 * 32 + (fq & 1) * 16 + (fq >> 1) * 8) = (u32x4){r0[0], r1[0], r0[1], r1[1]};
      }
    }
  } else {
    const float dv = bflo(dvraw);
    u32x4 qr[4], gx[2][2], gwv[2];
#pragma unroll
    for (int i = 0; i < 4; ++i) qr[i] = (u32x4){0u, 0u, 0u, 0u};
    gx[0][0] = gx[0][1] = gx[1][0] = gx[1][1] = gwv[0] = gwv[1] = (u32x4){0u, 0u, 0u, 0u};
    auto side_loads = [&]() AI {
#pragma unroll
    for (int i = 0; i < 4; ++i) qr[i] = *(const u32x4*)(q + (size_t)(rt * 128 + wid * 32 + i * 8 + (lane >> 3)) * D_ + ct * 64 + (lane & 7) * 8);
#pragma unroll
    for (int r = 0; r < 2; ++r) {
      const u16* xr = q + (size_t)(b * 8 + wid * 2 + r) * D_ + lane * 8;
      gx[r][0] = *(const u32x4*)xr; gx[r][1] = *(const u32x4*)(xr + 512);
    }
    gwv[0] = *(const u32x4*)(gw + lane * 8); gwv[1] = *(const u32x4*)(gw + 512 + lane * 8);
    };
    SCHED();
    conv_wave(lds + wid * CV_SIZE, in, dv, rws, ch, side_loads);
    {
      float sg2[2];
#pragma unroll
      for (int r = 0; r < 2; ++r) {
        float s = 0.f;
#pragma unroll
        for (int e = 0; e < 4; ++e) {
          s += rbf(bflo(gx[r][0][e]) * bflo(gwv[0][e])) + rbf(bfhi(gx[r][0][e]) * bfhi(gwv[0][e]));
          s += rbf(bflo(gx[r][1][e]) * bflo(gwv[1][e])) + rbf(bfhi(gx[r][1][e]) * bfhi(gwv[1][e]));
        }
#pragma unroll
        for (int o = 32; o >= 1; o >>= 1) s += __shfl_xor(s, o);
        sg2[r] = s;
      }
      const float s = rbf((lane & 1) ? sg2[1] : sg2[0]);
      const float g = rbf(1.0f / (1.0f + __expf(-s)));
      __builtin_amdgcn_raw_buffer_store_b32(__float_as_uint(g), rws, WS_G + (b * 8 + wid * 2 + (lane & 1)) * 4, 0, SC1);
    }
    WAIT_V(0);
    flag_set(ws, WS_FLAG + ((lane & 16) ? (unsigned)((b >> 4) * 16 + (lane & 15)) * FL_STRIDE + FL_G + ((b & 15) * 4 + wid) * 8
                                        : (unsigned)((lane & 15) * 16 + (ch >> 6)) * FL_STRIDE + FL_Z + (ch & 63) * 8), tag);
    *(volatile LAS unsigned*)(lds + L_CTRL + 32 + wid * 4) = 1u;
    LAS char* sg = lds + wid * CV_SIZE + L_STG;
    LAS char* zs = lds + wid * CV_SIZE + L_ZS;
#pragma unroll
    for (int i = 0; i < 4; ++i) *(LAS u32x4*)(sg + (i * 8 + (lane >> 3)) * 144 + (lane & 7) * 16) = qr[i];
    WAIT_L(0);
    u32x2 qv[2][4];
#pragma unroll
    for (int i2 = 0; i2 < 2; ++i2)
#pragma unroll
      for (int n = 0; n < 4; ++n) qv[i2][n] = *(LAS u32x2*)(sg + (i2 * 16 + fr) * 144 + n * 32 + fq * 8);
    lds_wait4<16>(lds + L_CTRL + 48);
    {
      const unsigned tl = (unsigned)tag, th = (unsigned)(tag >> 32);
      for (unsigned spins = 0; spins < (1u << 16); ++spins) {
        const u32x4 v = ld16(rws, lane * 16, myfl + FL_G);
        if (__all(v[0] == tl && v[1] == th && v[2] == tl && v[3] == th)) break;
        __builtin_amdgcn_s_sleep(16);
      }
      asm volatile("" ::: "memory");
      __builtin_amdgcn_fence(__ATOMIC_ACQUIRE, "wavefront");
    }
    float gv[2];
    {
      u32x4 zr[4];
#pragma unroll
      for (int i2 = 0; i2 < 2; ++i2) gv[i2] = __uint_as_float(__builtin_amdgcn_raw_buffer_load_b32(rws, (rt * 128 + (wid * 2 + i2) * 16 + fr) * 4, WS_G, SC1));
#pragma unroll
      for (int i = 0; i < 4; ++i) zr[i] = ld16(rws, (unsigned)(ct * 65 + i * 16 + (lane >> 2)) * 4096u + (lane & 3) * 16, WS_ZT + (unsigned)(rt * 128 + wid * 32) * 2u);
#pragma unroll
      for (int i = 0; i < 4; ++i) *(LAS u32x4*)(zs + (i * 16 + (lane >> 2)) * 80 + (lane & 3) * 16) = zr[i];
    }
    WAIT_L(0);
    const int q4 = (lane & 15) >> 2, p = lane & 3;
#pragma unroll
    for (int i2 = 0; i2 < 2; ++i2) {
      const float g = gv[i2];
#pragma unroll
      for (int n = 0; n < 4; ++n) {
        const s16x4 zz = __builtin_amdgcn_ds_read_tr16_b64_v4i16((LAS s16x4*)(zs + (16 * n + 4 * fq + q4) * 80 + (i2 * 16 + 4 * p) * 2));
        const u32x2 zu = __builtin_bit_cast(u32x2, zz);
        const unsigned q01 = qv[i2][n][0], q23 = qv[i2][n][1];
        f32x4 ga;
        ga[0] = rbf(g * rbf(bflo(q01) * bflo(zu[0]))); ga[1] = rbf(g * rbf(bfhi(q01) * bfhi(zu[0])));
        ga[2] = rbf(g * rbf(bflo(q23) * bflo(zu[1]))); ga[3] = rbf(g * rbf(bfhi(q23) * bfhi(zu[1])));
        *(LAS f32x4*)(sg + (i2 * 4 + n) * 1024 + lane * 16) = ga;
      }
      *(LAS float*)(sg + 8192 + i2 * 256 + lane * 4) = rbf(1.0f - g);
    }
    WAIT_L(0);
    BAR();
    if (wid < 3) *(u32x4*)(ws + myfl + (wid * 64 + lane) * 16) = (u32x4){0u, 0u, 0u, 0u};
  }
}

extern "C" void kernel_launch(void* const* d_in, const int* in_sizes, int n_in, void* d_out, int out_size, void* d_ws, size_t ws_size,
                              hipStream_t stream) {
  const u16* q = (const u16*)d_in[0]; const u16* kk = (const u16*)d_in[1]; const u16* vv = (const u16*)d_in[2];
  const u16* shk = (const u16*)d_in[3]; const u16* ssmk = (const u16*)d_in[4]; const u16* ssmD = (const u16*)d_in[5];
  const u16* wg = (const u16*)d_in[7]; const u16* wu = (const u16*)d_in[8]; const u16* wd = (const u16*)d_in[9];
  const u16* gw = (const u16*)d_in[10];
  constexpr int LDS1 = 163840;
  (void)hipFuncSetAttribute((const void*)gated_mix_fused_kernel, hipFuncAttributeMaxDynamicSharedMemorySize, LDS1);
  gated_mix_fused_kernel<<<256, 512, LDS1, stream>>>(q, kk, vv, shk, ssmk, ssmD, wg, wu, wd, gw, (char*)d_ws, (u16*)d_out);
}
```
